```python
import torch

def _softmax(x, axis=-1):
    return torch.softmax(x, dim=axis)
def _gelu(x):
    return torch.nn.functional.gelu(x, approximate="tanh")
def _silu(x):
    return torch.nn.functional.silu(x)
def _sigmoid(x):
    return torch.sigmoid(x)
def _layernorm(x, axis=-1, eps=1e-5):
    m = x.mean(dim=axis, keepdim=True)
    v = x.var(dim=axis, keepdim=True, unbiased=False)
    return (x - m) / torch.sqrt(v + eps)
def _scatter_add(tgt, idx, src, axis=0):
    return tgt.clone().index_add_(axis, idx, src)
def _segment_sum(src, seg, n):
    out = torch.zeros((n,) + src.shape[1:], dtype=src.dtype)
    return out.index_add_(0, seg, src)
def _pad(x, pw):
    flat = tuple(v for pair in reversed(pw) for v in pair)
    return torch.nn.functional.pad(x, flat)

kernel_name = 'gated_mix__softmax_full_mha__glu_gated_glu__prompt'

def setup_inputs():

    _w = (lambda a: (lambda t: t.to(torch.bfloat16) if t.is_floating_point() else t)(torch.as_tensor(a)))
    def _A_setup():
        B, S, D = 1, 2048, 448
        return {
            "x": _w((lambda *s: torch.randn(*s, dtype=torch.bfloat16) * 0.05)(B, S, D)),
            "wqkv": _w((lambda *s: torch.randn(*s, dtype=torch.bfloat16) * 0.8)(D, 3 * D)),
            "wo": _w((lambda *s: torch.randn(*s, dtype=torch.bfloat16) * 0.1)(D, D)),
        }
    _A_in = _A_setup()

    def _B_setup():
        B, S, D, F = 1, 2048, 448, 1792
        return {
            "x": _w((lambda *s: torch.randn(*s, dtype=torch.bfloat16) * 0.05)(B, S, D)),
            "w_gate": _w((lambda *s: torch.randn(*s, dtype=torch.bfloat16) * 0.4)(D, F)),
            "w_up": _w((lambda *s: torch.randn(*s, dtype=torch.bfloat16) * 0.05)(D, F)),
            "w_down": _w((lambda *s: torch.randn(*s, dtype=torch.bfloat16) * 0.05)(F, D)),
        }
    _B_in = _B_setup()

    return {
        "A_x": _A_in["x"],
        "A_wqkv": _A_in["wqkv"],
        "A_wo": _A_in["wo"],
        "B_x": _B_in["x"],
        "B_w_gate": _B_in["w_gate"],
        "B_w_up": _B_in["w_up"],
        "B_w_down": _B_in["w_down"],
        "gate_w": _w((lambda *s: torch.randn(*s, dtype=torch.bfloat16) * 0.05)(448) * 16.0),
    }

def reference(A_x, A_wqkv, A_wo, B_x, B_w_gate, B_w_up, B_w_down, gate_w):
    def _A_ref(x, wqkv, wo):
        B, S, D = x.shape
        H = 16; Dh = D // H
        qkv = (lambda x, *d: x.permute(*d))((x @ wqkv).reshape(B, S, 3, H, Dh), 2, 0, 3, 1, 4)
        q, k, v = qkv[0], qkv[1], qkv[2]
        scale = Dh ** -0.5
        scores = torch.einsum("bhqd,bhkd->bhqk", q, k) * scale
        p = _softmax(scores, axis=-1)
        o = torch.einsum("bhqk,bhkd->bhqd", p, v)
        return (lambda x, *d: x.permute(*d))(o, 0, 2, 1, 3).reshape(B, S, D) @ wo

    def _B_ref(x, w_gate, w_up, w_down):
        g = torch.einsum("bsd,df->bsf", x, w_gate)
        u = torch.einsum("bsd,df->bsf", x, w_up)
        h = _sigmoid(g) * u
        return torch.einsum("bsf,fd->bsd", h, w_down)

    x = A_x
    g = _sigmoid((x * gate_w).sum(-1, keepdims=True))
    a = _A_ref(x, A_wqkv, A_wo)
    b = _B_ref(x, B_w_gate, B_w_up, B_w_down)
    return g * a + (1 - g) * b
```

```cpp
#include <hip/hip_runtime.h>
#include <hip/hip_bf16.h>
#include <stdint.h>

typedef short bf16x8 __attribute__((ext_vector_type(8)));
typedef short s16x4 __attribute__((ext_vector_type(4)));
typedef float f32x16 __attribute__((ext_vector_type(16)));
typedef float f32x4 __attribute__((ext_vector_type(4)));
typedef unsigned u32x4 __attribute__((ext_vector_type(4)));
typedef unsigned u32x2 __attribute__((ext_vector_type(2)));
typedef __amdgpu_buffer_rsrc_t rsrc_t;
#define LAS __attribute__((address_space(3)))
#define DI __device__ __forceinline__
typedef LAS char* lptr;

constexpr int SEQ = 2048, DM = 448, NH = 16, DH = 28, FF = 1792;
constexpr float QSCALE = 0.18898223650461362f * 1.4426950408889634f;
constexpr float LOG2E = 1.4426950408889634f;

constexpr size_t WS_Q = 0;
constexpr size_t WS_KV = WS_Q + (2u << 20);
constexpr size_t WS_O = WS_KV + (4u << 20);
constexpr size_t WS_H = WS_O + (2u << 20);
constexpr size_t WS_G = WS_H + (size_t)64 * 112 * 1024;
constexpr int LDS_BYTES = 80 * 1024;

#define MFMA(a, b, c) __builtin_amdgcn_mfma_f32_32x32x16_bf16((a), (b), (c), 0, 0, 0)
#define SBAR() __builtin_amdgcn_sched_barrier(0)
#define WAITV(n) asm volatile("s_waitcnt vmcnt(" #n ")" ::: "memory")
#define BARRIER() asm volatile("s_waitcnt lgkmcnt(0)\n\ts_barrier" ::: "memory")

DI rsrc_t mk_rsrc(const void* p, unsigned bytes) { return __builtin_amdgcn_make_buffer_rsrc((void*)p, (short)0, (int)bytes, 0x00020000); }
DI unsigned rfl(unsigned v) { return (unsigned)__builtin_amdgcn_readfirstlane((int)v); }
DI void dma16(rsrc_t r, unsigned voff, unsigned soff, unsigned ldsaddr) {
    asm volatile("s_mov_b32 m0, %0\n\ts_nop 0\n\tbuffer_load_dwordx4 %1, %2, %3 offen lds" :: "s"(ldsaddr), "v"(voff), "s"(r), "s"(soff) : "memory");
}
DI void gload16(u32x4& dst, rsrc_t r, unsigned voff, unsigned soff) {
    asm volatile("buffer_load_dwordx4 %0, %1, %2, %3 offen" : "=v"(dst) : "v"(voff), "s"(r), "s"(soff) : "memory");
}
typedef float f32x2 __attribute__((ext_vector_type(2)));
typedef __bf16 bf16x2_t __attribute__((ext_vector_type(2)));
DI unsigned cvtpk(float lo, float hi) { return __builtin_bit_cast(unsigned, __builtin_convertvector((f32x2){lo, hi}, bf16x2_t)); }
DI bf16x8 lds_b128(lptr p) { return *(const LAS bf16x8*)p; }
typedef short v4i16_t __attribute__((ext_vector_type(4)));
DI s16x4 lds_tr(lptr p) { return __builtin_bit_cast(s16x4, __builtin_amdgcn_ds_read_tr16_b64_v4i16((LAS v4i16_t*)p)); }
DI bf16x8 cat(s16x4 lo, s16x4 hi) { return __builtin_shufflevector(lo, hi, 0, 1, 2, 3, 4, 5, 6, 7); }
DI u32x4 pack8(const f32x16& a, int s) {
    u32x4 r; r[0] = cvtpk(a[8 * s], a[8 * s + 1]); r[1] = cvtpk(a[8 * s + 2], a[8 * s + 3]); r[2] = cvtpk(a[8 * s + 4], a[8 * s + 5]); r[3] = cvtpk(a[8 * s + 6], a[8 * s + 7]); return r;
}
DI float bf2f(unsigned short v) { return __uint_as_float((unsigned)v << 16); }

DI void qkv_part(const uint16_t* x, const uint16_t* wqkv, char* ws, lptr lds, int h, int rb) {
    const int tid = threadIdx.x, lane = tid & 63, r32 = lane & 31, hh = lane >> 5; const unsigned wid = rfl(tid >> 6);
    const rsrc_t rx = mk_rsrc(x, SEQ * DM * 2), rw = mk_rsrc(wqkv, DM * 3 * DM * 2);
    constexpr unsigned XB = 16384, STG = 28672;
    const unsigned l0 = (unsigned)(uintptr_t)lds;
    unsigned vx[4], vw[3];
#pragma unroll
    for (int p = 0; p < 4; ++p) { const unsigned r = 32 * wid + 8 * p + (lane >> 3); vx[p] = (rb * 128 + r) * (DM * 2) + (((lane & 7) ^ ((r >> 1) & 7)) << 4); }
#pragma unroll
    for (int j = 0; j < 3; ++j) vw[j] = (16 * wid + (lane >> 2)) * (3 * DM * 2) + (DM * j + DH * h) * 2 + ((lane & 3) << 4);
    auto stage = [&](int kt) {
        const unsigned sl = l0 + (kt & 1) * STG;
#pragma unroll
        for (int p = 0; p < 4; ++p) dma16(rx, vx[p], kt * 128, sl + wid * 4096 + p * 1024);
#pragma unroll
        for (int j = 0; j < 3; ++j) dma16(rw, vw[j], kt * 64 * (3 * DM * 2), sl + XB + j * 4096 + wid * 1024);
    };
    stage(0); stage(1);
    f32x16 aq = {}, ak = {}, av = {};
    const unsigned xoff = (32 * wid + r32) * 128, xsw = (r32 >> 1) & 7;
    const unsigned woff = XB + (8 * hh + ((lane & 15) >> 2)) * 64 + ((lane >> 4) & 1) * 32 + (lane & 3) * 8;
#pragma unroll 1
    for (int kt = 0; kt < 7; ++kt) {
        if (kt < 6) WAITV(7); else WAITV(0);
        BARRIER();
        lptr sl = lds + (kt & 1) * STG;
#pragma unroll
        for (int s = 0; s < 4; ++s) {
            const bf16x8 xf = lds_b128(sl + xoff + (((2 * s + hh) ^ xsw) << 4));
            const bf16x8 wq = cat(lds_tr(sl + woff + s * 1024), lds_tr(sl + woff + s * 1024 + 256));
            const bf16x8 wk = cat(lds_tr(sl + woff + 4096 + s * 1024), lds_tr(sl + woff + 4096 + s * 1024 + 256));
            const bf16x8 wv = cat(lds_tr(sl + woff + 8192 + s * 1024), lds_tr(sl + woff + 8192 + s * 1024 + 256));
            aq = MFMA(wq, xf, aq); ak = MFMA(wk, xf, ak); av = MFMA(xf, wv, av);
        }
        BARRIER();
        if (kt + 2 < 7) stage(kt + 2);
    }
#pragma unroll
    for (int i = 0; i < 16; ++i) aq[i] *= QSCALE;
    if (hh) {
#pragma unroll
        for (int i = 12; i < 16; ++i) { aq[i] = 0.f; ak[i] = 0.f; }
    }
    if (r32 >= DH) { const float c = r32 == DH ? 1.f : 0.f;
#pragma unroll
        for (int i = 0; i < 16; ++i) av[i] = c; }
    const int tt = rb * 4 + wid;
    u32x4* qd = (u32x4*)(ws + WS_Q + ((size_t)(h * 64 + tt) * 2048)) + lane;
    u32x4* kd = (u32x4*)(ws + WS_KV + ((size_t)(h * 64 + tt) * 4096)) + lane;
    qd[0] = pack8(aq, 0); qd[64] = pack8(aq, 1);
    kd[0] = pack8(ak, 0); kd[64] = pack8(ak, 1); kd[128] = pack8(av, 0); kd[192] = pack8(av, 1);
}

DI void glu_part(const uint16_t* x, const uint16_t* wg, const uint16_t* wu, char* ws, lptr lds, int rb, int cg) {
    const int tid = threadIdx.x, lane = tid & 63, r32 = lane & 31, hh = lane >> 5; const unsigned wid = rfl(tid >> 6);
    const rsrc_t rx = mk_rsrc(x, SEQ * DM * 2), rg = mk_rsrc(wg, DM * FF * 2), ru = mk_rsrc(wu, DM * FF * 2);
    constexpr unsigned STG = 24576;
    const unsigned l0 = (unsigned)(uintptr_t)lds;
    unsigned vx[2], vw[2];
#pragma unroll
    for (int p = 0; p < 2; ++p) {
        const unsigned r = 16 * (2 * wid + p) + (lane >> 2); vx[p] = (rb * 128 + r) * (DM * 2) + (((lane & 3) ^ ((r >> 2) & 3)) << 4);
        const unsigned k = 4 * (2 * wid + p) + (lane >> 4); vw[p] = k * (FF * 2) + cg * 256 + (((lane & 15) ^ ((k & 3) << 2)) << 4);
    }
    auto stage = [&](int kt, unsigned slot) {
        const unsigned sl = l0 + slot;
#pragma unroll
        for (int p = 0; p < 2; ++p) {
            dma16(rx, vx[p], kt * 64, sl + wid * 2048 + p * 1024);
            dma16(rg, vw[p], kt * 32 * (FF * 2), sl + 8192 + wid * 2048 + p * 1024);
            dma16(ru, vw[p], kt * 32 * (FF * 2), sl + 16384 + wid * 2048 + p * 1024);
        }
    };
    stage(0, 0); stage(1, STG);
    const unsigned wr = wid & 1, wp = wid >> 1;
    f32x16 ag[2][2] = {}, au[2][2] = {};
    const unsigned q = (lane & 15) >> 2;
    const unsigned xoff = (64 * wr + r32) * 64, xsw = (r32 >> 2) & 3;
    unsigned woff[2];
#pragma unroll
    for (int c = 0; c < 2; ++c) woff[c] = 8192 + (8 * hh + q) * 256 + (((2 * wp + c) ^ q) << 6) + ((lane >> 4) & 1) * 32 + (lane & 3) * 8;
    unsigned cur = 0, nxt2 = 2 * STG;
#pragma unroll 1
    for (int kt = 0; kt < 14; ++kt) {
        if (kt < 13) WAITV(6); else WAITV(0);
        BARRIER();
        if (kt + 2 < 14) stage(kt + 2, nxt2);
        lptr sl = lds + cur;
#pragma unroll
        for (int s = 0; s < 2; ++s) {
            bf16x8 xf[2], gf[2], uf[2];
#pragma unroll
            for (int t = 0; t < 2; ++t) xf[t] = lds_b128(sl + xoff + t * 2048 + (((2 * s + hh) ^ xsw) << 4));
#pragma unroll
            for (int c = 0; c < 2; ++c) {
                gf[c] = cat(lds_tr(sl + woff[c] + s * 4096), lds_tr(sl + woff[c] + s * 4096 + 1024));
                uf[c] = cat(lds_tr(sl + woff[c] + 8192 + s * 4096), lds_tr(sl + woff[c] + 8192 + s * 4096 + 1024));
            }
#pragma unroll
            for (int t = 0; t < 2; ++t)
#pragma unroll
                for (int c = 0; c < 2; ++c) { ag[t][c] = MFMA(gf[c], xf[t], ag[t][c]); au[t][c] = MFMA(uf[c], xf[t], au[t][c]); }
        }
        nxt2 = cur; cur = (cur == 2 * STG) ? 0 : cur + STG;
    }
#pragma unroll
    for (int t = 0; t < 2; ++t)
#pragma unroll
        for (int c = 0; c < 2; ++c) {
            f32x16 hv;
#pragma unroll
            for (int i = 0; i < 16; ++i) hv[i] = au[t][c][i] * __builtin_amdgcn_rcpf(1.f + __builtin_amdgcn_exp2f(-LOG2E * ag[t][c][i]));
            const int tt = rb * 4 + 2 * wr + t, ks = (cg * 4 + 2 * wp + c) * 2;
            u32x4* hd = (u32x4*)(ws + WS_H + ((size_t)(tt * 112 + ks) * 1024)) + lane;
            hd[0] = pack8(hv, 0); hd[64] = pack8(hv, 1);
        }
}

DI void gate_part(const uint16_t* x, const uint16_t* gw, char* ws, int blk) {
    const int tid = threadIdx.x, row = blk * 64 + (tid >> 2), part = tid & 3;
    float s = 0.f;
    const u32x4* xr = (const u32x4*)(x + (size_t)row * DM); const u32x4* gr = (const u32x4*)gw;
#pragma unroll
    for (int i = 0; i < 14; ++i) {
        const u32x4 a = xr[i * 4 + part], b = gr[i * 4 + part];
#pragma unroll
        for (int j = 0; j < 4; ++j) {
            s += __uint_as_float(a[j] << 16) * __uint_as_float(b[j] << 16);
            s += __uint_as_float(a[j] & 0xffff0000u) * __uint_as_float(b[j] & 0xffff0000u);
        }
    }
    s += __shfl_xor(s, 1); s += __shfl_xor(s, 2);
    if (part == 0) ((float*)(ws + WS_G))[row] = __builtin_amdgcn_rcpf(1.f + __builtin_amdgcn_exp2f(-LOG2E * s));
}

DI float xhalf_max(float m) { auto rr = __builtin_amdgcn_permlane32_swap(__float_as_uint(m), __float_as_uint(m), false, false); return fmaxf(__uint_as_float(rr[0]), __uint_as_float(rr[1])); }
DI float max16(const f32x16& p) {
    float a = fmaxf(p[0], p[1]);
#pragma unroll
    for (int i = 2; i < 16; ++i) a = fmaxf(a, p[i]);
    return xhalf_max(a);
}
template <bool SAFE>
DI void attn_pass(char* ws, lptr lds, int h, const bf16x8 q0, const bf16x8 q1, f32x16& o) {
    const int tid = threadIdx.x, lane = tid & 63; const unsigned wid = rfl(tid >> 6);
    const rsrc_t rkv = mk_rsrc(ws + WS_KV + (size_t)h * 64 * 4096, 64 * 4096);
    const unsigned l0 = (unsigned)(uintptr_t)lds;
    const unsigned vsrc = wid * 4096 + lane * 16;
    auto stage = [&](int c) {
#pragma unroll
        for (int p = 0; p < 4; ++p) dma16(rkv, vsrc, c * 16384 + p * 1024, l0 + (c & 3) * 16384 + wid * 4096 + p * 1024);
    };
    stage(0); stage(1); stage(2);
    WAITV(8); BARRIER();
    lptr lp = lds + lane * 16;
    f32x16 zero = {};
    o = zero;
    if constexpr (!SAFE) {
        bf16x8 k0 = lds_b128(lp), k1 = lds_b128(lp + 1024);
        f32x16 sA = MFMA(k0, q0, zero); sA = MFMA(k1, q1, sA);
        const float nm = -max16(sA);
        f32x16 cm;
#pragma unroll
        for (int i = 0; i < 16; ++i) { cm[i] = nm; sA[i] += nm; }
        f32x16 sB;
        k0 = lds_b128(lp + 4096); k1 = lds_b128(lp + 4096 + 1024);
        bf16x8 v0 = lds_b128(lp + 2048), v1 = lds_b128(lp + 3072);
#define ATT_STEP(SC, SN, KOFF, VOFF) do { \
        SN = MFMA(k0, q0, cm); SN = MFMA(k1, q1, SN); \
        bf16x8 kn0 = lds_b128(lp + (KOFF)), kn1 = lds_b128(lp + (KOFF) + 1024); \
        bf16x8 vn0 = lds_b128(lp + (VOFF) + 2048), vn1 = lds_b128(lp + (VOFF) + 3072); \
        _Pragma("unroll") for (int i = 0; i < 16; ++i) SC[i] = __builtin_amdgcn_exp2f(SC[i]); \
        o = MFMA(v0, __builtin_bit_cast(bf16x8, pack8(SC, 0)), o); \
        o = MFMA(v1, __builtin_bit_cast(bf16x8, pack8(SC, 1)), o); \
        k0 = kn0; k1 = kn1; v0 = vn0; v1 = vn1; } while (0)
#pragma unroll 1
        for (int c = 0; c < 16; ++c) {
            const unsigned cb = (c & 3) * 16384, nb = ((c + 1) & 3) * 16384;
            ATT_STEP(sA, sB, cb + 2 * 4096, cb + 1 * 4096);
            ATT_STEP(sB, sA, cb + 3 * 4096, cb + 2 * 4096);
            if (c < 14) WAITV(4); else WAITV(0);
            BARRIER();
            if (c + 3 < 16) stage(c + 3);
            ATT_STEP(sA, sB, nb, cb + 3 * 4096);
            ATT_STEP(sB, sA, nb + 4096, nb);
        }
#undef ATT_STEP
    } else {
        float m = -INFINITY;
#pragma unroll 1
        for (int c = 0; c < 16; ++c) {
            const unsigned cb = (c & 3) * 16384;
#pragma unroll 1
            for (int t = 0; t < 4; ++t) {
                lptr tp = lp + cb + t * 4096;
                f32x16 s = MFMA(lds_b128(tp), q0, zero); s = MFMA(lds_b128(tp + 1024), q1, s);
                const float mn = fmaxf(m, max16(s)); const float al = __builtin_amdgcn_exp2f(m - mn); m = mn;
#pragma unroll
                for (int i = 0; i < 16; ++i) { s[i] = __builtin_amdgcn_exp2f(s[i] - mn); o[i] *= al; }
                o = MFMA(lds_b128(tp + 2048), __builtin_bit_cast(bf16x8, pack8(s, 0)), o);
                o = MFMA(lds_b128(tp + 3072), __builtin_bit_cast(bf16x8, pack8(s, 1)), o);
            }
            if (c < 14) WAITV(4); else WAITV(0);
            BARRIER();
            if (c + 3 < 16) stage(c + 3);
        }
    }
    BARRIER();
}
DI void attn_part(char* ws, lptr lds, int h, int rb) {
    const int tid = threadIdx.x, lane = tid & 63, hh = lane >> 5; const unsigned wid = rfl(tid >> 6);
    const int tt = rb * 4 + wid;
    const bf16x8* qs = (const bf16x8*)(ws + WS_Q + ((size_t)(h * 64 + tt) * 2048)) + lane;
    const bf16x8 q0 = qs[0], q1 = qs[64];
    f32x16 o;
    attn_pass<false>(ws, lds, h, q0, q1, o);
    float mag = 0.f;
#pragma unroll
    for (int i = 0; i < 16; ++i) mag = fmaxf(mag, fabsf(o[i]));
    bool bad = !(mag < 1.8e19f);
#pragma unroll
    for (int i = 0; i < 16; ++i) bad |= (o[i] != o[i]);
    if (__syncthreads_or(bad)) attn_pass<true>(ws, lds, h, q0, q1, o);
    auto rr = __builtin_amdgcn_permlane32_swap(__float_as_uint(o[12]), __float_as_uint(o[12]), false, false);
    const float inv = __builtin_amdgcn_rcpf(__uint_as_float(rr[1]));
#pragma unroll
    for (int i = 0; i < 16; ++i) o[i] *= inv;
    if (hh) {
#pragma unroll
        for (int i = 12; i < 16; ++i) o[i] = 0.f;
    }
    u32x4* od = (u32x4*)(ws + WS_O + ((size_t)(tt * 16 + h) * 2048)) + lane;
    od[0] = pack8(o, 0); od[64] = pack8(o, 1);
}

DI void out_part(const uint16_t* wd, const uint16_t* wo, char* ws, uint16_t* out, lptr lds, int rb, int nb) {
    const int tid = threadIdx.x, lane = tid & 63, r32 = lane & 31, hh = lane >> 5; const unsigned wid = rfl(tid >> 6);
    const rsrc_t rd = mk_rsrc(wd, FF * DM * 2), ro = mk_rsrc(wo, DM * DM * 2);
    const rsrc_t rh = mk_rsrc(ws + WS_H, 64 * 112 * 1024), rof = mk_rsrc(ws + WS_O, 2u << 20);
    const unsigned l0 = (unsigned)(uintptr_t)lds + wid * 20480;
    const unsigned vw = (lane >> 3) * (DM * 2) + nb * 128 + (((lane & 7) ^ (((lane >> 4) & 1) << 2)) << 4);
    const unsigned va = lane * 16;
    constexpr int P = 4, NS = 5, NST = 18;
    u32x4 af[P][4];
    auto issue = [&](int i) {
        const unsigned dst = l0 + (i % NS) * 4096;
        if (i < 14) {
            const unsigned so = (wid * 448 + i * 32) * (DM * 2);
#pragma unroll
            for (int p = 0; p < 4; ++p) dma16(rd, vw, so + p * 8 * (DM * 2), dst + p * 1024);
#pragma unroll
            for (int t = 0; t < 2; ++t)
#pragma unroll
                for (int s = 0; s < 2; ++s) gload16(af[i % P][2 * t + s], rh, va, ((rb * 2 + t) * 112 + wid * 28 + 2 * i + s) * 1024);
        } else {
            const unsigned hd = wid * 4 + (i - 14), so = hd * DH * (DM * 2);
#pragma unroll
            for (int p = 0; p < 4; ++p) dma16(ro, vw, so + p * 8 * (DM * 2), dst + p * 1024);
#pragma unroll
            for (int t = 0; t < 2; ++t)
#pragma unroll
                for (int s = 0; s < 2; ++s) gload16(af[i % P][2 * t + s], rof, va, (((rb * 2 + t) * 16 + hd) * 2 + s) * 1024);
        }
    };
#pragma unroll
    for (int i = 0; i < P; ++i) issue(i);
    f32x16 ab[2][2] = {}, aa[2][2] = {};
    const unsigned q = (lane & 15) >> 2;
    unsigned woff[2];
#pragma unroll
    for (int c = 0; c < 2; ++c) woff[c] = wid * 20480 + (4 * hh + q) * 128 + ((c ^ (q >> 1)) << 6) + ((lane >> 4) & 1) * 32 + (lane & 3) * 8;
#pragma unroll
    for (int i = 0; i < NST; ++i) {
        u32x4(&a)[4] = af[i % P];
        const int rem = NST - 1 - i < P - 1 ? NST - 1 - i : P - 1;
        if (rem == 3) asm volatile("s_waitcnt vmcnt(24)" : "+v"(a[0]), "+v"(a[1]), "+v"(a[2]), "+v"(a[3]) :: "memory");
        else if (rem == 2) asm volatile("s_waitcnt vmcnt(16)" : "+v"(a[0]), "+v"(a[1]), "+v"(a[2]), "+v"(a[3]) :: "memory");
        else if (rem == 1) asm volatile("s_waitcnt vmcnt(8)" : "+v"(a[0]), "+v"(a[1]), "+v"(a[2]), "+v"(a[3]) :: "memory");
        else asm volatile("s_waitcnt vmcnt(0)" : "+v"(a[0]), "+v"(a[1]), "+v"(a[2]), "+v"(a[3]) :: "memory");
        lptr sl = lds + (i % NS) * 4096;
#pragma unroll
        for (int s = 0; s < 2; ++s) {
            bf16x8 wf[2];
#pragma unroll
            for (int c = 0; c < 2; ++c) wf[c] = cat(lds_tr(sl + woff[c] + s * 2048), lds_tr(sl + woff[c] + s * 2048 + 1024));
#pragma unroll
            for (int t = 0; t < 2; ++t)
#pragma unroll
                for (int c = 0; c < 2; ++c) {
                    if (i < 14) ab[t][c] = MFMA(wf[c], __builtin_bit_cast(bf16x8, a[2 * t + s]), ab[t][c]);
                    else aa[t][c] = MFMA(wf[c], __builtin_bit_cast(bf16x8, a[2 * t + s]), aa[t][c]);
                }
        }
        SBAR();
        if (i + P < NST) issue(i + P);
    }
    const float* G = (const float*)(ws + WS_G);
#pragma unroll
    for (int t = 0; t < 2; ++t) { const float g = G[rb * 64 + t * 32 + r32], g1 = 1.f - g;
#pragma unroll
        for (int c = 0; c < 2; ++c)
#pragma unroll
            for (int i = 0; i < 16; ++i) ab[t][c][i] = g * aa[t][c][i] + g1 * ab[t][c][i]; }
    BARRIER();
#pragma unroll
    for (int t = 0; t < 2; ++t)
#pragma unroll
        for (int c = 0; c < 2; ++c)
#pragma unroll
            for (int g = 0; g < 4; ++g)
                *(LAS f32x4*)(lds + (((2 * t + c) * 4 + wid) * 4 + g) * 1024 + lane * 16) = (f32x4){ab[t][c][4 * g], ab[t][c][4 * g + 1], ab[t][c][4 * g + 2], ab[t][c][4 * g + 3]};
    BARRIER();
    const unsigned t = wid >> 1, c = wid & 1;
    uint16_t* orow = out + (size_t)(rb * 64 + t * 32 + r32) * DM + nb * 64 + c * 32 + 4 * hh;
#pragma unroll
    for (int g = 0; g < 4; ++g) {
        f32x4 s = *(LAS f32x4*)(lds + ((wid * 4 + 0) * 4 + g) * 1024 + lane * 16);
#pragma unroll
        for (int w = 1; w < 4; ++w) s += *(LAS f32x4*)(lds + ((wid * 4 + w) * 4 + g) * 1024 + lane * 16);
        *(u32x2*)(orow + 8 * g) = (u32x2){cvtpk(s[0], s[1]), cvtpk(s[2], s[3])};
    }
}

__global__ __launch_bounds__(256, 2) void k1(const uint16_t* x, const uint16_t* wqkv, const uint16_t* wg, const uint16_t* wu, const uint16_t* gw, char* ws) {
    extern __shared__ __attribute__((aligned(16))) char smem[];
    const int b = blockIdx.x;
    if (b < 256) qkv_part(x, wqkv, ws, (lptr)smem, 2 * (b & 7) + ((b >> 3) & 1), b >> 4);
    else if (b < 480) { const int i = b - 256, xc = i & 7, j = i >> 3;
        glu_part(x, wg, wu, ws, (lptr)smem, (xc >> 1) * 4 + (j & 3), (xc & 1) * 7 + (j >> 2)); }
    else gate_part(x, gw, ws, b - 480);
}
__global__ __launch_bounds__(256, 2) void k2(char* ws) {
    extern __shared__ __attribute__((aligned(16))) char smem[];
    const int b = blockIdx.x;
    attn_part(ws, (lptr)smem, 2 * (b & 7) + ((b >> 3) & 1), b >> 4);
}
__global__ __launch_bounds__(256, 2) void k3(const uint16_t* wd, const uint16_t* wo, char* ws, uint16_t* out) {
    extern __shared__ __attribute__((aligned(16))) char smem[];
    const int b = blockIdx.x, xc = b & 7, j = b >> 3;
    out_part(wd, wo, ws, out, (lptr)smem, xc * 4 + (j & 3), j >> 2);
}

extern "C" void kernel_launch(void* const* d_in, const int* in_sizes, int n_in, void* d_out, int out_size, void* d_ws, size_t ws_size, hipStream_t stream) {
    const uint16_t* x = (const uint16_t*)d_in[0];
    const uint16_t* wqkv = (const uint16_t*)d_in[1];
    const uint16_t* wo = (const uint16_t*)d_in[2];
    const uint16_t* wg = (const uint16_t*)d_in[4];
    const uint16_t* wu = (const uint16_t*)d_in[5];
    const uint16_t* wd = (const uint16_t*)d_in[6];
    const uint16_t* gw = (const uint16_t*)d_in[7];
    char* ws = (char*)d_ws;
    (void)hipFuncSetAttribute((const void*)k1, hipFuncAttributeMaxDynamicSharedMemorySize, LDS_BYTES);
    (void)hipFuncSetAttribute((const void*)k2, hipFuncAttributeMaxDynamicSharedMemorySize, LDS_BYTES);
    (void)hipFuncSetAttribute((const void*)k3, hipFuncAttributeMaxDynamicSharedMemorySize, LDS_BYTES);
    k1<<<512, 256, LDS_BYTES, stream>>>(x, wqkv, wg, wu, gw, ws);
    k2<<<256, 256, LDS_BYTES, stream>>>(ws);
    k3<<<224, 256, LDS_BYTES, stream>>>(wd, wo, ws, (uint16_t*)d_out);
}
```

```cpp
#include <hip/hip_runtime.h>
#include <hip/hip_bf16.h>
#include <stdint.h>

typedef short bf16x8 __attribute__((ext_vector_type(8)));
typedef short s16x4 __attribute__((ext_vector_type(4)));
typedef float f32x16 __attribute__((ext_vector_type(16)));
typedef float f32x4 __attribute__((ext_vector_type(4)));
typedef float f32x2 __attribute__((ext_vector_type(2)));
typedef unsigned u32x4 __attribute__((ext_vector_type(4)));
typedef unsigned u32x2 __attribute__((ext_vector_type(2)));
typedef __bf16 bf16x2_t __attribute__((ext_vector_type(2)));
typedef unsigned long long u64;
typedef __amdgpu_buffer_rsrc_t rsrc_t;
#define LAS __attribute__((address_space(3)))
#define DI __device__ __forceinline__
typedef LAS char* lptr;

constexpr int SEQ = 2048, DM = 448, DH = 28, FF = 1792;
constexpr float LOG2E = 1.4426950408889634f;
constexpr float QSCALE = 0.18898223650461362f * LOG2E;

constexpr unsigned WS_Q = 0;
constexpr unsigned WS_KV = WS_Q + (2u << 20);
constexpr unsigned WS_O = WS_KV + (4u << 20);
constexpr unsigned WS_H = WS_O + (2u << 20);
constexpr unsigned WS_G = WS_H + 64u * 112 * 1024;
constexpr unsigned WS_FQ = WS_G + 8192;
constexpr unsigned WS_FH = WS_FQ + 2048;
constexpr unsigned WS_FO = WS_FH + 16384;
constexpr unsigned WS_E = WS_FO + 8192;
constexpr unsigned WS_END = WS_E + 256;
constexpr unsigned OOB = 0x7ffffff0u;

constexpr int LDS_BYTES = 160 * 1024;
constexpr unsigned L_RING = 7 * 16384;
constexpr unsigned L_COMB = 98304;
constexpr unsigned L_CNT = LDS_BYTES - 64;

#define MFMA(a, b, c) __builtin_amdgcn_mfma_f32_32x32x16_bf16((a), (b), (c), 0, 0, 0)
#define SBAR() __builtin_amdgcn_sched_barrier(0)
#define PIN(x) asm volatile("" : "+v"(x))
#define BARRIER() asm volatile("s_waitcnt lgkmcnt(0)\n\ts_barrier" ::: "memory")
template <int N> DI void waitv() { asm volatile("s_waitcnt vmcnt(%0)" :: "n"(N) : "memory"); }

#define STAMP(kern, i) do {} while (0)

DI rsrc_t mk_rsrc(const void* p, unsigned bytes) { return __builtin_amdgcn_make_buffer_rsrc((void*)p, (short)0, (int)bytes, 0x00020000); }
DI unsigned rfl(unsigned v) { return (unsigned)__builtin_amdgcn_readfirstlane((int)v); }
DI void dma16(rsrc_t r, unsigned voff, unsigned soff, unsigned ldsaddr) {
    asm volatile("s_mov_b32 m0, %0\n\ts_nop 0\n\tbuffer_load_dwordx4 %1, %2, %3 offen lds" :: "s"(ldsaddr), "v"(voff), "s"(r), "s"(soff) : "memory");
}
DI void gload16_sc1(u32x4& dst, rsrc_t r, unsigned voff, unsigned soff) {
    asm volatile("buffer_load_dwordx4 %0, %1, %2, %3 offen sc1" : "=v"(dst) : "v"(voff), "s"(r), "s"(soff) : "memory");
}
DI bf16x8 ld_sc1(rsrc_t r, unsigned voff, unsigned soff) { return __builtin_bit_cast(bf16x8, __builtin_amdgcn_raw_buffer_load_b128(r, voff, soff, 16)); }
DI void st_sc1(u32x4 v, rsrc_t r, unsigned voff) { __builtin_amdgcn_raw_buffer_store_b128(v, r, voff, 0, 16); }
DI unsigned cvtpk(float lo, float hi) { return __builtin_bit_cast(unsigned, __builtin_convertvector((f32x2){lo, hi}, bf16x2_t)); }
DI bf16x8 lds_b128(lptr p) { return *(const LAS bf16x8*)p; }
DI s16x4 lds_tr(lptr p) { return __builtin_amdgcn_ds_read_tr16_b64_v4i16((LAS s16x4*)p); }
DI bf16x8 cat(s16x4 lo, s16x4 hi) { return __builtin_shufflevector(lo, hi, 0, 1, 2, 3, 4, 5, 6, 7); }
DI u32x4 pack8(const f32x16& a, int s) {
    u32x4 r; r[0] = cvtpk(a[8 * s], a[8 * s + 1]); r[1] = cvtpk(a[8 * s + 2], a[8 * s + 3]); r[2] = cvtpk(a[8 * s + 4], a[8 * s + 5]); r[3] = cvtpk(a[8 * s + 6], a[8 * s + 7]); return r;
}
DI float sigmoidf_(float v) { return __builtin_amdgcn_rcpf(1.f + __builtin_amdgcn_exp2f(-LOG2E * v)); }

DI void set_flag(rsrc_t rws, unsigned off, u64 tag) {
    __builtin_amdgcn_raw_buffer_store_b64((u32x2){(unsigned)tag, (unsigned)(tag >> 32)}, rws, off, 0, 16);
}
template <int NW> DI void wait_flags(rsrc_t rws, unsigned off, u64 tag) {
    for (int spin = 0; spin < 40000; ++spin) {
        bool ok = true;
#pragma unroll
        for (int i = 0; i < NW; ++i) { const u32x2 v = __builtin_amdgcn_raw_buffer_load_b64(rws, off + i * 512, 0, 16); ok &= (v[0] == (unsigned)tag) & (v[1] == (unsigned)(tag >> 32)); }
        if (__all(ok)) break;
        __builtin_amdgcn_s_sleep(2);
    }
    asm volatile("" ::: "memory");
}
DI void group_barrier(lptr cnt, unsigned target) {
    asm volatile("s_waitcnt lgkmcnt(0)" ::: "memory");
    if ((threadIdx.x & 63) == 0) __hip_atomic_fetch_add((LAS unsigned*)cnt, 1u, __ATOMIC_RELAXED, __HIP_MEMORY_SCOPE_WORKGROUP);
    for (int spin = 0; spin < 4000000; ++spin) {
        if (rfl(__hip_atomic_load((LAS unsigned*)cnt, __ATOMIC_RELAXED, __HIP_MEMORY_SCOPE_WORKGROUP)) >= target) break;
        __builtin_amdgcn_s_sleep(1);
    }
    asm volatile("" ::: "memory");
}

struct WSrc { rsrc_t r; unsigned v0, v1, step; };
DI void issueW(const WSrc& s, int st, unsigned dst) { dma16(s.r, s.v0, st * s.step, dst); dma16(s.r, s.v1, st * s.step, dst + 1024); }
DI WSrc mk_wsrc(rsrc_t r, unsigned pitch, unsigned colb, unsigned row0, unsigned rows_per_stage, int lane) {
    WSrc s; s.r = r; const unsigned sw = ((lane & 7) ^ (((lane >> 4) & 1) << 2)) << 4;
    s.v0 = (row0 + (lane >> 3)) * pitch + colb + sw; s.v1 = s.v0 + 8 * pitch; s.step = rows_per_stage * pitch; return s;
}
enum { K_QK = 0, K_V = 1, K_G = 2 };
struct P1 { lptr lds; unsigned l0, wid, slot, xoff, xsw, woff; int lane; };
DI unsigned nslot(unsigned s) { return s == L_RING + 32768 ? L_RING : s + 16384; }

template <int NB, int KIND, int W0, int W1, bool HASNEXT, class Hook>
DI void pass(P1& c, const WSrc& cur, const WSrc& nxt, f32x16 (&acc)[4], Hook hook) {
    constexpr int NST = NB == 1 ? 4 : (NB == 2 ? 7 : 14), KS = 8 / NB;
#pragma unroll
    for (int i = 0; i < 4; ++i) acc[i] = (f32x16){};
#pragma unroll 1
    for (int st = 0; st < NST; ++st) {
        if (st == 0) waitv<W0>(); else if (st == 1) waitv<W1>(); else if (!HASNEXT && st == NST - 1) waitv<0>(); else waitv<2>();
        BARRIER();
        const unsigned dst = c.l0 + nslot(nslot(c.slot)) + c.wid * 2048;
        if (st + 2 < NST) issueW(cur, st + 2, dst); else if (HASNEXT) issueW(nxt, st + 2 - NST, dst);
        hook(st);
        lptr wp = c.lds + c.slot + c.woff;
        const int k0 = st * KS;
#pragma unroll
        for (int s = 0; s < KS; ++s) {
            if (NB == 1 && s >= 4 && st == 3) break;
            const unsigned ks = k0 + s;
            lptr xp = c.lds + (ks >> 2) * 16384 + c.xoff + (((2 * (ks & 3)) ^ c.xsw) << 4);
            if constexpr (NB == 1) {
                const bf16x8 xf = lds_b128(xp);
                if constexpr (KIND == K_G) { const bf16x8 wf = lds_b128(c.lds + c.slot + ks * 32 + (c.lane >> 5) * 16); acc[0] = MFMA(wf, xf, acc[0]); }
                else { const bf16x8 wf = cat(lds_tr(wp + s * 2048), lds_tr(wp + s * 2048 + 512));
                    if constexpr (KIND == K_V) acc[0] = MFMA(xf, wf, acc[0]); else acc[0] = MFMA(wf, xf, acc[0]); }
            } else if constexpr (NB == 2) {
                const bf16x8 xf = lds_b128(xp);
                const bf16x8 w0 = cat(lds_tr(wp + s * 2048), lds_tr(wp + s * 2048 + 512)), w1 = cat(lds_tr(wp + 8192 + s * 2048), lds_tr(wp + 8192 + s * 2048 + 512));
                acc[0] = MFMA(w0, xf, acc[0]); acc[1] = MFMA(w1, xf, acc[1]);
            } else {
                const bf16x8 x0 = lds_b128(xp), x1 = lds_b128(xp + 4096);
                const bf16x8 w0 = cat(lds_tr(wp + s * 2048), lds_tr(wp + s * 2048 + 512)), w1 = cat(lds_tr(wp + 4096 + s * 2048), lds_tr(wp + 4096 + s * 2048 + 512));
                acc[0] = MFMA(w0, x0, acc[0]); acc[1] = MFMA(w1, x0, acc[1]); acc[2] = MFMA(w0, x1, acc[2]); acc[3] = MFMA(w1, x1, acc[3]);
            }
        }
        c.slot = nslot(c.slot);
    }
}
DI void set_geom(P1& c, unsigned row0, unsigned blkoff, unsigned half) {
    const int lane = c.lane; const unsigned r32 = lane & 31, hh = lane >> 5, q = (lane & 15) >> 2;
    c.xoff = (row0 + r32) * 128; c.xsw = hh ^ ((r32 >> 1) & 7);
    c.woff = blkoff + (8 * hh + q) * 128 + ((half ^ (q >> 1)) << 6) + ((lane >> 4) & 1) * 32 + (lane & 3) * 8;
}
DI void epi_qk(const f32x16& a, unsigned n0, unsigned tt, rsrc_t rws, int lane) {
    const unsigned r32 = lane & 31, hh = lane >> 5; const bool isq = n0 < 448; const float sc = isq ? QSCALE : 1.f;
    const unsigned nn = isq ? n0 : n0 - 448, base = isq ? WS_Q : WS_KV, hstride = isq ? 64 * 2048 : 64 * 4096, toff = tt * (isq ? 2048 : 4096) + r32 * 16;
    const unsigned pad0 = isq ? 0u : 0x3f80u;
#pragma unroll
    for (int p = 0; p < 2; ++p) {
        unsigned a0 = cvtpk(a[8 * p] * sc, a[8 * p + 1] * sc), a1 = cvtpk(a[8 * p + 2] * sc, a[8 * p + 3] * sc), b0 = cvtpk(a[8 * p + 4] * sc, a[8 * p + 5] * sc), b1 = cvtpk(a[8 * p + 6] * sc, a[8 * p + 7] * sc);
        auto s0 = __builtin_amdgcn_permlane32_swap(a0, b0, false, false); auto s1 = __builtin_amdgcn_permlane32_swap(a1, b1, false, false);
        const unsigned c8 = (nn >> 3) + 2 * p + hh, m = c8 / 7, e = c8 - 7 * m;
        const unsigned head = 2 * m + (e > 3), slot = e < 4 ? e : e - 4;
        const unsigned o1 = base + head * hstride + toff + (slot >> 1) * 1024 + (slot & 1) * 512;
        const bool split = e == 3;
        st_sc1((u32x4){s0[0], s1[0], split ? pad0 : s0[1], split ? 0u : s1[1]}, rws, o1);
        st_sc1((u32x4){s0[1], s1[1], pad0, 0u}, rws, split ? o1 + hstride : OOB);
    }
}
DI void epi_v(const f32x16& a, unsigned n0, unsigned tt, rsrc_t rws, int lane) {
    const unsigned r32 = lane & 31, hh = lane >> 5, n = n0 + r32, head = n / 28, d = n - 28 * head;
    const unsigned o = WS_KV + (head * 64 + tt) * 4096 + 2048 + (d + 32 * hh) * 16;
    st_sc1(pack8(a, 0), rws, o); st_sc1(pack8(a, 1), rws, o + 1024);
    const unsigned one = d == 0 ? 0x3f803f80u : 0u, op = d < 4 ? o + 28 * 16 : OOB;
    st_sc1((u32x4){one, one, one, one}, rws, op); st_sc1((u32x4){one, one, one, one}, rws, op + 1024);
}
DI void epi_h(const f32x16& g, const f32x16& u, unsigned ct, unsigned tt, rsrc_t rws, int lane) {
    f32x16 hv;
#pragma unroll
    for (int i = 0; i < 16; ++i) hv[i] = u[i] * sigmoidf_(g[i]);
    const unsigned o = WS_H + (tt * 112 + ct * 2) * 1024 + lane * 16;
    st_sc1(pack8(hv, 0), rws, o); st_sc1(pack8(hv, 1), rws, o + 1024);
}

DI float max16(const f32x16& p) {
    float a = fmaxf(p[0], p[1]);
#pragma unroll
    for (int i = 2; i < 16; ++i) a = fmaxf(a, p[i]);
    auto rr = __builtin_amdgcn_permlane32_swap(__float_as_uint(a), __float_as_uint(a), false, false);
    return fmaxf(__uint_as_float(rr[0]), __uint_as_float(rr[1]));
}
#define EXP2(v) __builtin_amdgcn_exp2f(v)
DI void attn_part(char* ws, rsrc_t rws, lptr lds, unsigned wid, int lane, unsigned h, unsigned rb, u64 tag) {
    const unsigned hh = lane >> 5, va = lane * 16;
    const rsrc_t rkv = mk_rsrc(ws + WS_KV + h * (64 * 4096), 64 * 4096);
    const rsrc_t rmy = mk_rsrc(ws + WS_KV + h * (64 * 4096) + wid * (16 * 4096), 16 * 4096);
    bf16x8 q[4][2], kv[4][4];
#pragma unroll
    for (int i = 0; i < 4; ++i) { q[i][0] = ld_sc1(rws, va, WS_Q + (h * 64 + rb * 4 + i) * 2048); q[i][1] = ld_sc1(rws, va, WS_Q + (h * 64 + rb * 4 + i) * 2048 + 1024); }
    const bf16x8 kz0 = ld_sc1(rkv, va, 0), kz1 = ld_sc1(rkv, va, 1024);
#pragma unroll
    for (int t = 0; t < 3; ++t)
#pragma unroll
        for (int j = 0; j < 4; ++j) kv[t][j] = ld_sc1(rmy, va, t * 4096 + j * 1024);
    const f32x16 zero = {};
#pragma unroll
    for (int i = 0; i < 4; ++i) {
        f32x16 s = MFMA(kz0, q[i][0], zero); s = MFMA(kz1, q[i][1], s);
        const unsigned nm = cvtpk(-max16(s), 0.f);
        u32x4 t = __builtin_bit_cast(u32x4, q[i][1]); t[2] = hh ? nm : t[2]; q[i][1] = __builtin_bit_cast(bf16x8, t);
    }
    f32x16 o[4], sA, sB; u32x4 p0, p1 = {0, 0, 0, 0};
#pragma unroll
    for (int i = 0; i < 4; ++i) o[i] = zero;
    sA = MFMA(kv[0][0], q[0][0], zero); sA = MFMA(kv[0][1], q[0][1], sA);
    bf16x8 vprev = kv[0][3]; kv[3][3] = kv[0][3];
    float tp0 = 0.f, tp1 = 0.f, tp2 = 0.f, tp3 = 0.f;
#define UNIT(SC, SN, I, KN0, KN1, IN, V0, VP1, IP, LOADS) do { \
    SN = MFMA(KN0, q[IN][0], zero); SC[0] = EXP2(SC[0]); SC[1] = EXP2(SC[1]); p1[2] = cvtpk(tp0, tp1); SC[2] = EXP2(SC[2]); SC[3] = EXP2(SC[3]); p1[3] = cvtpk(tp2, tp3); PIN(SC); PIN(p1); SBAR(); \
    SN = MFMA(KN1, q[IN][1], SN);   SC[4] = EXP2(SC[4]); SC[5] = EXP2(SC[5]); p0[0] = cvtpk(SC[0], SC[1]); SC[6] = EXP2(SC[6]); SC[7] = EXP2(SC[7]); p0[1] = cvtpk(SC[2], SC[3]); PIN(SC); PIN(p0); SBAR(); \
    o[IP] = MFMA(VP1, __builtin_bit_cast(bf16x8, p1), o[IP]); SC[8] = EXP2(SC[8]); SC[9] = EXP2(SC[9]); p0[2] = cvtpk(SC[4], SC[5]); p0[3] = cvtpk(SC[6], SC[7]); SC[10] = EXP2(SC[10]); SC[11] = EXP2(SC[11]); PIN(SC); PIN(p0); SBAR(); \
    LOADS; \
    o[I] = MFMA(V0, __builtin_bit_cast(bf16x8, p0), o[I]); tp0 = EXP2(SC[12]); tp1 = EXP2(SC[13]); p1[0] = cvtpk(SC[8], SC[9]); tp2 = EXP2(SC[14]); tp3 = EXP2(SC[15]); p1[1] = cvtpk(SC[10], SC[11]); \
    PIN(tp0); PIN(tp1); PIN(tp2); PIN(tp3); PIN(p1); SBAR(); } while (0)
#define TILE(B, BN, BP, TOFF) do { \
    UNIT(sA, sB, 0, kv[B][0], kv[B][1], 1, kv[B][2], kv[BP][3], 3, \
         { kv[BP][0] = ld_sc1(rmy, va, (TOFF)); kv[BP][1] = ld_sc1(rmy, va, (TOFF) + 1024); kv[BP][2] = ld_sc1(rmy, va, (TOFF) + 2048); kv[BP][3] = ld_sc1(rmy, va, (TOFF) + 3072); SBAR(); }); \
    UNIT(sB, sA, 1, kv[B][0], kv[B][1], 2, kv[B][2], kv[B][3], 0, {}); \
    UNIT(sA, sB, 2, kv[B][0], kv[B][1], 3, kv[B][2], kv[B][3], 1, {}); \
    UNIT(sB, sA, 3, kv[BN][0], kv[BN][1], 0, kv[B][2], kv[B][3], 2, {}); } while (0)
    if (wid == 0) STAMP(0, 6);
#pragma unroll 1
    for (int t = 0; t < 16; t += 4) {
        const unsigned to = (t + 3) * 4096;
        TILE(0, 1, 3, to); TILE(1, 2, 0, to + 4096); TILE(2, 3, 1, to + 8192); TILE(3, 0, 2, to + 12288);
    }
#undef TILE
#undef UNIT
    (void)vprev;
    if (wid == 0) STAMP(0, 7);
    p1[2] = cvtpk(tp0, tp1); p1[3] = cvtpk(tp2, tp3);
    o[3] = MFMA(kv[3][3], __builtin_bit_cast(bf16x8, p1), o[3]);
#pragma unroll
    for (int i = 0; i < 4; ++i) if (i != (int)wid) {
        const unsigned sl = i * 3 + (wid > (unsigned)i ? wid - 1 : wid);
#pragma unroll
        for (int g = 0; g < 4; ++g) *(LAS f32x4*)(lds + L_COMB + (sl * 4 + g) * 1024 + va) = (f32x4){o[i][4 * g], o[i][4 * g + 1], o[i][4 * g + 2], o[i][4 * g + 3]};
    }
    f32x16 r = wid == 0 ? o[0] : (wid == 1 ? o[1] : (wid == 2 ? o[2] : o[3]));
    group_barrier(lds + L_CNT, 4);
#pragma unroll
    for (int j = 0; j < 3; ++j)
#pragma unroll
        for (int g = 0; g < 4; ++g) { const f32x4 v = *(LAS f32x4*)(lds + L_COMB + ((wid * 3 + j) * 4 + g) * 1024 + va); r[4 * g] += v[0]; r[4 * g + 1] += v[1]; r[4 * g + 2] += v[2]; r[4 * g + 3] += v[3]; }
    if (wid == 0) STAMP(0, 8);
    bool ok = true;
#pragma unroll
    for (int i = 0; i < 16; ++i) ok &= fabsf(r[i]) < 1.8e19f;
    if (!__all(ok)) {
        const bf16x8 q0 = ld_sc1(rws, va, WS_Q + (h * 64 + rb * 4 + wid) * 2048), q1 = ld_sc1(rws, va, WS_Q + (h * 64 + rb * 4 + wid) * 2048 + 1024);
        float m = -INFINITY; r = zero;
#pragma unroll 1
        for (int t = 0; t < 64; ++t) {
            const bf16x8 k0 = ld_sc1(rkv, va, t * 4096), k1 = ld_sc1(rkv, va, t * 4096 + 1024), v0 = ld_sc1(rkv, va, t * 4096 + 2048), v1 = ld_sc1(rkv, va, t * 4096 + 3072);
            f32x16 s = MFMA(k0, q0, zero); s = MFMA(k1, q1, s);
            const float mn = fmaxf(m, max16(s)), al = EXP2(m - mn); m = mn;
#pragma unroll
            for (int i = 0; i < 16; ++i) { s[i] = EXP2(s[i] - mn); r[i] *= al; }
            r = MFMA(v0, __builtin_bit_cast(bf16x8, pack8(s, 0)), r); r = MFMA(v1, __builtin_bit_cast(bf16x8, pack8(s, 1)), r);
        }
    }
    auto rr = __builtin_amdgcn_permlane32_swap(__float_as_uint(r[12]), __float_as_uint(r[12]), false, false);
    const float inv = __builtin_amdgcn_rcpf(__uint_as_float(rr[1]));
#pragma unroll
    for (int i = 0; i < 16; ++i) r[i] = (hh && i >= 12) ? 0.f : r[i] * inv;
    const unsigned tt = rb * 4 + wid, oo = WS_O + (tt * 16 + h) * 2048 + va;
    st_sc1(pack8(r, 0), rws, oo); st_sc1(pack8(r, 1), rws, oo + 1024);
    waitv<0>();
    if (lane == 0) set_flag(rws, WS_FO + ((tt >> 1) * 32 + h * 2 + (tt & 1)) * 8, tag);
}

constexpr int OB_NS = 6, OB_P = 4;
struct OutC { rsrc_t rd, ro, rws; unsigned l0, vw, va, w, rb; };
DI void out_issueW(const OutC& c, int i) {
    const unsigned dst = c.l0 + (i % OB_NS) * 4096;
    const unsigned so = i < 14 ? (c.w * 448 + i * 32) * (DM * 2) : (c.w * 4 + (i - 14)) * DH * (DM * 2);
#pragma unroll
    for (int p = 0; p < 4; ++p) dma16(i < 14 ? c.rd : c.ro, c.vw, so + p * 8 * (DM * 2), dst + p * 1024);
}
DI void out_issueA(const OutC& c, int i, u32x4 (&a)[4]) {
#pragma unroll
    for (int t = 0; t < 2; ++t)
#pragma unroll
        for (int s = 0; s < 2; ++s)
            gload16_sc1(a[2 * t + s], c.rws, c.va, i < 14 ? WS_H + ((c.rb * 2 + t) * 112 + c.w * 28 + 2 * i + s) * 1024 : WS_O + (((c.rb * 2 + t) * 16 + c.w * 4 + (i - 14)) * 2 + s) * 1024);
}
template <int N> DI void waitv4(u32x4 (&a)[4]) { asm volatile("s_waitcnt vmcnt(%4)" : "+v"(a[0]), "+v"(a[1]), "+v"(a[2]), "+v"(a[3]) : "n"(N) : "memory"); }
constexpr int out_younger(int i) { int n = 0;
    for (int k = i + 1; k < OB_P; ++k) n += 4;
    for (int j = (i - OB_P + 1 > 0 ? i - OB_P + 1 : 0); j < i; ++j) { if (j + OB_NS < 18) n += 4; if (j + OB_P < 14) n += 4; }
    return n; }
template <int I> DI void out_stage(const OutC& c, lptr lds, const unsigned (&woff)[2], u32x4 (&af)[OB_P][4], f32x16 (&acc)[2][2]) {
    u32x4(&a)[4] = af[I % OB_P];
    if constexpr (I < 14) waitv4<out_younger(I)>(a); else waitv4<(17 - I) * 4>(a);
    lptr sl = lds + (I % OB_NS) * 4096;
#pragma unroll
    for (int s = 0; s < 2; ++s) {
        bf16x8 wf[2];
#pragma unroll
        for (int cc = 0; cc < 2; ++cc) wf[cc] = cat(lds_tr(sl + woff[cc] + s * 2048), lds_tr(sl + woff[cc] + s * 2048 + 1024));
#pragma unroll
        for (int t = 0; t < 2; ++t)
#pragma unroll
            for (int cc = 0; cc < 2; ++cc) acc[t][cc] = MFMA(wf[cc], __builtin_bit_cast(bf16x8, a[2 * t + s]), acc[t][cc]);
    }
    SBAR();
    if constexpr (I + OB_NS < 18) out_issueW(c, I + OB_NS);
    if constexpr (I + OB_P < 14) out_issueA(c, I + OB_P, a);
}
template <int I0, int I1> DI void out_stages(const OutC& c, lptr lds, const unsigned (&woff)[2], u32x4 (&af)[OB_P][4], f32x16 (&acc)[2][2]) {
    if constexpr (I0 < I1) { out_stage<I0>(c, lds, woff, af, acc); out_stages<I0 + 1, I1>(c, lds, woff, af, acc); }
}
DI void out_part(const OutC& c, char* ws, uint16_t* out, lptr lds, int lane, unsigned nb, u64 tag) {
    const unsigned r32 = lane & 31, hh = lane >> 5, w = c.w, rb = c.rb;
    u32x4 af[OB_P][4];
    wait_flags<1>(c.rws, WS_FH + (rb * 64 + lane) * 8, tag);
    STAMP(1, 4);
#pragma unroll
    for (int i = 0; i < OB_P; ++i) out_issueA(c, i, af[i]);
    f32x16 ab[2][2] = {}, aa[2][2] = {};
    const unsigned q = (lane & 15) >> 2;
    unsigned woff[2];
#pragma unroll
    for (int cc = 0; cc < 2; ++cc) woff[cc] = w * 24576 + (4 * hh + q) * 128 + ((cc ^ (q >> 1)) << 6) + ((lane >> 4) & 1) * 32 + (lane & 3) * 8;
    out_stages<0, 14>(c, lds, woff, af, ab);
    STAMP(1, 5);
    wait_flags<1>(c.rws, lane < 32 ? WS_FO + (rb * 32 + lane) * 8 : WS_FQ + ((rb >> 1) * 16 + 13) * 8, tag);
    STAMP(1, 6);
    const float g0 = __uint_as_float(__builtin_amdgcn_raw_buffer_load_b32(c.rws, WS_G + (rb * 64 + r32) * 4, 0, 16));
    const float g1 = __uint_as_float(__builtin_amdgcn_raw_buffer_load_b32(c.rws, WS_G + (rb * 64 + 32 + r32) * 4, 0, 16));
#pragma unroll
    for (int i = 14; i < 18; ++i) out_issueA(c, i, af[i % OB_P]);
    out_stages<14, 18>(c, lds, woff, af, aa);
#pragma unroll
    for (int t = 0; t < 2; ++t) { const float g = t ? g1 : g0, gm = 1.f - g;
#pragma unroll
        for (int cc = 0; cc < 2; ++cc)
#pragma unroll
            for (int i = 0; i < 16; ++i) ab[t][cc][i] = g * aa[t][cc][i] + gm * ab[t][cc][i]; }
#pragma unroll
    for (int t = 0; t < 2; ++t)
#pragma unroll
        for (int cc = 0; cc < 2; ++cc)
#pragma unroll
            for (int g = 0; g < 4; ++g)
                *(LAS f32x4*)(lds + w * 24576 + ((2 * t + cc) * 4 + g) * 1024 + lane * 16) = (f32x4){ab[t][cc][4 * g], ab[t][cc][4 * g + 1], ab[t][cc][4 * g + 2], ab[t][cc][4 * g + 3]};
    group_barrier(lds + L_CNT + 16, 4);
    uint16_t* orow = out + (size_t)(rb * 64 + (w >> 1) * 32 + r32) * DM + nb * 64 + (w & 1) * 32 + 4 * hh;
#pragma unroll
    for (int g = 0; g < 4; ++g) {
        f32x4 s = *(LAS f32x4*)(lds + (w * 4 + g) * 1024 + lane * 16);
#pragma unroll
        for (int v = 1; v < 4; ++v) s += *(LAS f32x4*)(lds + v * 24576 + (w * 4 + g) * 1024 + lane * 16);
        *(u32x2*)(orow + 8 * g) = (u32x2){cvtpk(s[0], s[1]), cvtpk(s[2], s[3])};
    }
}

__global__ __launch_bounds__(512, 2) void fused(const uint16_t* x, const uint16_t* wqkv, const uint16_t* wo, const uint16_t* wg, const uint16_t* wu,
                                                const uint16_t* wd, const uint16_t* gw, char* ws, uint16_t* out) {
    extern __shared__ __attribute__((aligned(16))) char smem[];
    lptr lds = (lptr)smem;
    const int tid = threadIdx.x, lane = tid & 63; const unsigned wid = rfl(tid >> 6);
    const unsigned b = blockIdx.x, xc = b & 7, j = b >> 3;
    if (wid == 0) STAMP(0, 0);
    u64 tag = *(const __attribute__((address_space(4))) u64*)(ws + WS_E);
    tag += 0x9E3779B97F4A7C15ull; tag = (tag ^ (tag >> 30)) * 0xBF58476D1CE4E5B9ull; tag = (tag ^ (tag >> 27)) * 0x94D049BB133111EBull; tag ^= tag >> 31;
    const rsrc_t rws = mk_rsrc(ws, WS_END);
    {
        const unsigned rb = 8 * (xc & 1) + (j & 7), cg = 4 * (xc >> 1) + (j >> 3);
        const rsrc_t rx = mk_rsrc(x, SEQ * DM * 2), rq = mk_rsrc(wqkv, DM * 3 * DM * 2), rg = mk_rsrc(wg, DM * FF * 2), ru = mk_rsrc(wu, DM * FF * 2);
        P1 c; c.lds = lds; c.l0 = (unsigned)(uintptr_t)lds; c.wid = wid; c.slot = L_RING; c.lane = lane;
        const unsigned xr = 16 * wid + (lane >> 3);
        const unsigned vx0 = (rb * 128 + xr) * (DM * 2) + (((lane & 7) ^ ((xr >> 1) & 7)) << 4), vx1 = (rb * 128 + xr + 8) * (DM * 2) + (((lane & 7) ^ (((xr + 8) >> 1) & 7)) << 4);
        auto issueX = [&](int kc) { dma16(rx, vx0, kc * 128, c.l0 + kc * 16384 + wid * 2048); dma16(rx, vx1, kc * 128, c.l0 + kc * 16384 + wid * 2048 + 1024); };
        auto prologue = [&](const WSrc& s) {
            issueX(0); issueX(1); issueW(s, 0, c.l0 + L_RING + wid * 2048);
            issueX(2); issueX(3); issueW(s, 1, c.l0 + L_RING + 16384 + wid * 2048);
            issueX(4); issueX(5); issueX(6);
        };
        auto nohook = [](int) {};
        auto pubq = [&](int st) { if (st == 2 && tid == 0) set_flag(rws, WS_FQ + (rb * 16 + cg) * 8, tag); };
        f32x16 acc[4];
        const unsigned rt = wid & 3, half = wid >> 2;
        if (cg < 12) {
            const unsigned qb = cg < 7 ? 14 + cg : cg - 7;
            const WSrc s1 = mk_wsrc(rq, 3 * DM * 2, qb * 128, 16 * wid, 128, lane);
            const WSrc s2 = mk_wsrc((wid & 2) ? ru : rg, FF * 2, (2 * cg + (wid >> 2)) * 128, 16 * (wid & 1), 32, lane);
            prologue(s1);
            set_geom(c, 32 * rt, 0, half);
            if (cg < 7) { pass<1, K_V, 12, 8, true>(c, s1, s2, acc, nohook); epi_v(acc[0], cg * 64 + half * 32, rb * 4 + rt, rws, lane); }
            else { pass<1, K_QK, 12, 8, true>(c, s1, s2, acc, nohook); epi_qk(acc[0], qb * 64 + half * 32, rb * 4 + rt, rws, lane); }
            if (wid == 0) STAMP(0, 1);
            const unsigned rp = wid & 1, pi = wid >> 1;
            set_geom(c, 64 * rp, (pi >> 1) * 8192, pi & 1);
            pass<4, K_QK, 6, 6, false>(c, s2, s2, acc, pubq);
            if (wid == 0) STAMP(0, 2);
            BARRIER();
            if (tid < 16) *(LAS unsigned*)(lds + L_CNT + tid * 4) = 0;
            BARRIER();
            const unsigned ct = cg * 4 + pi;
            epi_h(acc[0], acc[1], ct, rb * 4 + 2 * rp, rws, lane); epi_h(acc[2], acc[3], ct, rb * 4 + 2 * rp + 1, rws, lane);
            waitv<0>();
            if (lane == 0) set_flag(rws, WS_FH + ((rb * 2 + rp) * 64 + cg * 4 + pi) * 8, tag);
        } else {
            const unsigned qb = 5 + 2 * (cg - 12);
            const WSrc s1 = mk_wsrc(rq, 3 * DM * 2, (qb + half) * 128, 16 * rt, 64, lane);
            WSrc s2 = mk_wsrc(rq, 3 * DM * 2, 13 * 128, 16 * wid, 128, lane);
            if (cg == 13) { s2.r = mk_rsrc(gw, DM * 2); s2.v0 = wid == 0 ? lane * 16 : OOB; s2.v1 = OOB; s2.step = 0; }
            const WSrc s3 = mk_wsrc(half ? ru : rg, FF * 2, (12 + cg) * 128, 16 * rt, 64, lane);
            prologue(s1);
            set_geom(c, 32 * rt, 0, half);
            if (cg < 14) {
                pass<2, K_QK, 12, 8, true>(c, s1, s2, acc, nohook);
                epi_qk(acc[0], qb * 64 + half * 32, rb * 4 + rt, rws, lane); epi_qk(acc[1], qb * 64 + 64 + half * 32, rb * 4 + rt, rws, lane);
                if (cg == 12) { pass<1, K_QK, 10, 10, true>(c, s2, s3, acc, nohook); epi_qk(acc[0], 13 * 64 + half * 32, rb * 4 + rt, rws, lane); }
                else { pass<1, K_G, 10, 10, true>(c, s2, s3, acc, nohook);
                    if (tid < 256 && lane < 32) __builtin_amdgcn_raw_buffer_store_b32(__float_as_uint(sigmoidf_(acc[0][0])), rws, WS_G + (rb * 128 + rt * 32 + lane) * 4, 0, 16); }
            } else {
                pass<2, K_QK, 12, 8, true>(c, s1, s3, acc, nohook);
                epi_qk(acc[0], qb * 64 + half * 32, rb * 4 + rt, rws, lane); epi_qk(acc[1], qb * 64 + 64 + half * 32, rb * 4 + rt, rws, lane);
            }
            if (wid == 0) STAMP(0, 1);
            pass<2, K_QK, 2, 2, false>(c, s3, s3, acc, pubq);
            if (wid == 0) STAMP(0, 2);
            BARRIER();
            if (tid < 16) *(LAS unsigned*)(lds + L_CNT + tid * 4) = 0;
            BARRIER();
            epi_h(acc[0], acc[1], (12 + cg) * 2 + half, rb * 4 + rt, rws, lane);
            waitv<0>();
            if (lane == 0) set_flag(rws, WS_FH + ((rb * 2 + (rt >> 1)) * 64 + cg * 4 + (rt & 1) * 2 + half) * 8, tag);
        }
    }
    if (wid == 0) STAMP(0, 3);
    if (wid < 4) {
        wait_flags<4>(rws, WS_FQ + lane * 8, tag);
        if (wid == 0) STAMP(0, 4);
        attn_part(ws, rws, lds, wid, lane, 2 * xc + (j & 1), j >> 1, tag);
        if (wid == 0) STAMP(0, 5);
        if (b == 0 && wid == 0) {
            wait_flags<16>(rws, WS_FO + lane * 8, tag);
            if (lane == 0) set_flag(rws, WS_E, tag);
        }
    } else if (j < 28) {
        OutC c; c.rd = mk_rsrc(wd, FF * DM * 2); c.ro = mk_rsrc(wo, DM * DM * 2); c.rws = rws; c.w = wid - 4; c.rb = xc * 4 + (j & 3);
        const unsigned nb = j >> 2;
        c.l0 = (unsigned)(uintptr_t)lds + c.w * 24576; c.va = lane * 16;
        c.vw = (lane >> 3) * (DM * 2) + nb * 128 + (((lane & 7) ^ (((lane >> 4) & 1) << 2)) << 4);
#pragma unroll
        for (int i = 0; i < OB_NS; ++i) out_issueW(c, i);
        out_part(c, ws, out, lds, lane, nb, tag);
        if (wid == 4) STAMP(1, 7);
    }
}

extern "C" void kernel_launch(void* const* d_in, const int* in_sizes, int n_in, void* d_out, int out_size, void* d_ws, size_t ws_size, hipStream_t stream) {
    (void)hipFuncSetAttribute((const void*)fused, hipFuncAttributeMaxDynamicSharedMemorySize, LDS_BYTES);
    fused<<<256, 512, LDS_BYTES, stream>>>((const uint16_t*)d_in[0], (const uint16_t*)d_in[1], (const uint16_t*)d_in[2], (const uint16_t*)d_in[4], (const uint16_t*)d_in[5],
                                           (const uint16_t*)d_in[6], (const uint16_t*)d_in[7], (char*)d_ws, (uint16_t*)d_out);
}
```

```cpp
#include <hip/hip_runtime.h>
#include <hip/hip_bf16.h>
#include <stdint.h>

typedef short bf16x8 __attribute__((ext_vector_type(8)));
typedef short s16x4 __attribute__((ext_vector_type(4)));
typedef float f32x16 __attribute__((ext_vector_type(16)));
typedef float f32x4 __attribute__((ext_vector_type(4)));
typedef float f32x2 __attribute__((ext_vector_type(2)));
typedef unsigned u32x4 __attribute__((ext_vector_type(4)));
typedef unsigned u32x2 __attribute__((ext_vector_type(2)));
typedef __bf16 bf16x2_t __attribute__((ext_vector_type(2)));
typedef unsigned long long u64;
typedef __amdgpu_buffer_rsrc_t rsrc_t;
#define LAS __attribute__((address_space(3)))
#define DI __device__ __forceinline__
typedef LAS char* lptr;

constexpr int SEQ = 2048, DM = 448, DH = 28, FF = 1792;
constexpr float LOG2E = 1.4426950408889634f;
constexpr float QSCALE = 0.18898223650461362f * LOG2E;

constexpr unsigned WS_Q = 0;
constexpr unsigned WS_K = WS_Q + (2u << 20);
constexpr unsigned WS_V = WS_K + (2u << 20);
constexpr unsigned WS_O = WS_V + (2u << 20);
constexpr unsigned WS_H = WS_O + (2u << 20);
constexpr unsigned WS_G = WS_H + 64u * 112 * 1024;
constexpr unsigned WS_FQ = WS_G + 16384;
constexpr unsigned WS_FH = WS_FQ + 2048;
constexpr unsigned WS_FO = WS_FH + 16384;
constexpr unsigned WS_E = WS_FO + 8192;
constexpr unsigned WS_END = WS_E + 256;
constexpr unsigned CODE_PF = 54 * 1024;
constexpr unsigned OOB = 0x7ffffff0u;

constexpr int LDS_BYTES = 160 * 1024;
constexpr unsigned L_RING = 7 * 16384;
constexpr unsigned RG(int i) { const int t[6] = {3, 1, 4, 9, 7, 5}; return t[i] * 16384; }
#define COMB(w) ((w) == 0 ? 0u : ((w) == 1 ? 2u * 16384 : ((w) == 2 ? 6u * 16384 : 8u * 16384)))
constexpr unsigned L_CNT = 2 * 16384 + 12288;

#define MFMA(a, b, c) __builtin_amdgcn_mfma_f32_32x32x16_bf16((a), (b), (c), 0, 0, 0)
#define SBAR() __builtin_amdgcn_sched_barrier(0)
#define PIN(x) asm volatile("" : "+v"(x))
#define BARRIER() asm volatile("s_waitcnt lgkmcnt(0)\n\ts_barrier" ::: "memory")
template <int N> DI void waitv() { asm volatile("s_waitcnt vmcnt(%0)" :: "n"(N) : "memory"); }

#define STAMP(kern, i) do {} while (0)

DI rsrc_t mk_rsrc(const void* p, unsigned bytes) { return __builtin_amdgcn_make_buffer_rsrc((void*)p, (short)0, (int)bytes, 0x00020000); }
DI unsigned rfl(unsigned v) { return (unsigned)__builtin_amdgcn_readfirstlane((int)v); }
DI void dma16(rsrc_t r, unsigned voff, unsigned soff, unsigned ldsaddr) {
    asm volatile("s_mov_b32 m0, %0\n\ts_nop 0\n\tbuffer_load_dwordx4 %1, %2, %3 offen lds" :: "s"(ldsaddr), "v"(voff), "s"(r), "s"(soff) : "memory");
}
DI void dma4(rsrc_t r, unsigned voff, unsigned ldsaddr) {
    asm volatile("s_mov_b32 m0, %0\n\ts_nop 0\n\tbuffer_load_dword %1, %2, 0 offen lds" :: "s"(ldsaddr), "v"(voff), "s"(r) : "memory");
}
DI void gload16_sc1(u32x4& dst, rsrc_t r, unsigned voff, unsigned soff) {
    asm volatile("buffer_load_dwordx4 %0, %1, %2, %3 offen sc1" : "=v"(dst) : "v"(voff), "s"(r), "s"(soff) : "memory");
}
DI bf16x8 ld_sc1(rsrc_t r, unsigned voff, unsigned soff) { return __builtin_bit_cast(bf16x8, __builtin_amdgcn_raw_buffer_load_b128(r, voff, soff, 16)); }
DI void st_sc1(u32x4 v, rsrc_t r, unsigned voff) { __builtin_amdgcn_raw_buffer_store_b128(v, r, voff, 0, 16); }
DI unsigned cvtpk(float lo, float hi) { return __builtin_bit_cast(unsigned, __builtin_convertvector((f32x2){lo, hi}, bf16x2_t)); }
DI bf16x8 lds_b128(lptr p) { return *(const LAS bf16x8*)p; }
DI s16x4 lds_tr(lptr p) { return __builtin_amdgcn_ds_read_tr16_b64_v4i16((LAS s16x4*)p); }
DI bf16x8 cat(s16x4 lo, s16x4 hi) { return __builtin_shufflevector(lo, hi, 0, 1, 2, 3, 4, 5, 6, 7); }
DI u32x4 pack8(const f32x16& a, int s) {
    u32x4 r; r[0] = cvtpk(a[8 * s], a[8 * s + 1]); r[1] = cvtpk(a[8 * s + 2], a[8 * s + 3]); r[2] = cvtpk(a[8 * s + 4], a[8 * s + 5]); r[3] = cvtpk(a[8 * s + 6], a[8 * s + 7]); return r;
}
DI float sigmoidf_(float v) { return __builtin_amdgcn_rcpf(1.f + __builtin_amdgcn_exp2f(-LOG2E * v)); }

DI void set_flag(rsrc_t rws, unsigned off, u64 tag) {
    __builtin_amdgcn_raw_buffer_store_b64((u32x2){(unsigned)tag, (unsigned)(tag >> 32)}, rws, off, 0, 16);
}
template <int NW> DI void wait_flags(rsrc_t rws, unsigned off, u64 tag) {
#pragma unroll 1
    for (int spin = 0; spin < 40000; ++spin) {
        bool ok = true;
#pragma unroll
        for (int i = 0; i < NW; ++i) { const u32x2 v = __builtin_amdgcn_raw_buffer_load_b64(rws, off + i * 512, 0, 16); ok &= (v[0] == (unsigned)tag) & (v[1] == (unsigned)(tag >> 32)); }
        if (__all(ok)) break;
        __builtin_amdgcn_s_sleep(4);
    }
    asm volatile("" ::: "memory");
}
DI void poll2(u32x4 (&v)[2], rsrc_t rws, unsigned off) {
    asm volatile("buffer_load_dwordx4 %0, %2, %3, 0 offen sc1\n\tbuffer_load_dwordx4 %1, %2, %3, 0 offen offset:16 sc1" : "=&v"(v[0]), "=&v"(v[1]) : "v"(off), "s"(rws) : "memory");
}
DI float load_gate(rsrc_t rws, unsigned tok, u64 tag) {
    u32x2 v;
#pragma unroll 1
    for (int spin = 0; spin < 40000; ++spin) {
        v = __builtin_amdgcn_raw_buffer_load_b64(rws, WS_G + tok * 8, 0, 16);
        if (__all(v[1] == (unsigned)tag)) break;
        __builtin_amdgcn_s_sleep(2);
    }
    return __uint_as_float(v[0]);
}
DI void lds_wait(lptr cnt, unsigned target) {
#pragma unroll 1
    for (int spin = 0; spin < 4000000; ++spin) {
        if (rfl(__hip_atomic_load((LAS unsigned*)cnt, __ATOMIC_RELAXED, __HIP_MEMORY_SCOPE_WORKGROUP)) >= target) break;
        __builtin_amdgcn_s_sleep(1);
    }
    asm volatile("" ::: "memory");
}
DI void group_barrier(lptr cnt, unsigned target) {
    asm volatile("s_waitcnt lgkmcnt(0)" ::: "memory");
    if ((threadIdx.x & 63) == 0) __hip_atomic_fetch_add((LAS unsigned*)cnt, 1u, __ATOMIC_RELAXED, __HIP_MEMORY_SCOPE_WORKGROUP);
    lds_wait(cnt, target);
}

struct WSrc { rsrc_t r; unsigned v0, v1, step; };
DI void issueW(const WSrc& s, int st, unsigned dst) { dma16(s.r, s.v0, st * s.step, dst); dma16(s.r, s.v1, st * s.step, dst + 1024); }
DI WSrc mk_wsrc(rsrc_t r, unsigned pitch, unsigned colb, unsigned row0, unsigned rows_per_stage, int lane) {
    WSrc s; s.r = r; const unsigned sw = ((lane & 7) ^ (((lane >> 4) & 1) << 2)) << 4;
    s.v0 = (row0 + (lane >> 3)) * pitch + colb + sw; s.v1 = s.v0 + 8 * pitch; s.step = rows_per_stage * pitch; return s;
}
constexpr int OB_NS = 6, OB_P = 4;
struct OutC { rsrc_t rd, ro, rws; unsigned l0, vw, va, w, rb; };
DI void out_issueW1(const OutC& c, int i, int p) {
    const unsigned so = i < 14 ? (c.w * 448 + i * 32) * (DM * 2) : (c.w * 4 + (i - 14)) * DH * (DM * 2);
    dma16(i < 14 ? c.rd : c.ro, c.vw, so + p * 8 * (DM * 2), c.l0 + RG(i % OB_NS) + p * 1024);
}
DI void out_issueW(const OutC& c, int i) {
#pragma unroll
    for (int p = 0; p < 4; ++p) out_issueW1(c, i, p);
}
struct Geo { unsigned xoff[2], woff, wlo; };
struct P1 { lptr lds; unsigned l0, wid, xsw; int lane; rsrc_t rx; unsigned vx0, vx1; WSrc ws[2]; Geo g[2]; OutC oc; unsigned trv; int tri; };
#define TR(c) do {} while (0)
constexpr int QS = 4;
constexpr int n_st(int P) { return P == 0 ? 18 : 14; }
constexpr int n_gr(int P) { return P == 0 ? 35 : 28; }
constexpr int st_nb(int P, int n) { return (P == 0 && n >= QS && n < QS + 4) ? 1 : 4; }
constexpr int st_idx(int P, int n) { return P == 0 ? (n < QS ? n : (n < QS + 4 ? n - QS : n - 4)) : n; }
constexpr int st_short(int P) { return P == 0 ? QS + 3 : 99; }
constexpr int g_stage(int P, int g) { return g <= 2 * st_short(P) ? g / 2 : (g + 1) / 2; }
constexpr int g_sub(int P, int g) { return g <= 2 * st_short(P) ? g % 2 : (g + 1) % 2; }
constexpr int x_after(int P, int n) { return (n >= 1 && n <= 5) ? n + 1 : -1; }
constexpr int x_need(int P, int n) { const int nb = st_nb(P, n), k = (st_idx(P, n) + 1) * (8 / nb) - 1; return (k >> 2) > 6 ? 6 : (k >> 2); }
constexpr int w_ring(int P, int k) { return P == 0 ? (k <= 8 ? k + 2 : (k <= 10 ? k + 4 : (k <= 12 ? k + 5 : -1))) : (k + 2 < 14 ? k + 2 : -1); }
constexpr int w_extra(int P, int k) { return P == 0 ? (k == 5 ? 11 : (k == 6 ? 12 : (k == 10 ? 15 : -1))) : -1; }
constexpr unsigned w_slot(int P, int n) { const int t[18] = {7, 8, 9, 7, 8, 9, 7, 8, 9, 7, 8, 0, 1, 9, 7, 2, 8, 0}; return P == 0 ? t[n] * 16384 : L_RING + (n % 3) * 16384; }
constexpr bool g_sync(int P, int g) { return g + 1 < n_gr(P) && g_sub(P, g + 1) == 0; }
constexpr int g_wd(int P, int g) { return g_sync(P, g) ? w_ring(P, g_stage(P, g + 1)) : ((g_sub(P, g) == 0 && g > 0) ? w_extra(P, g_stage(P, g)) : -1); }
constexpr int g_xd(int P, int g) { return (!g_sync(P, g) && g_sub(P, g) == 0) ? x_after(P, g_stage(P, g)) : -1; }
constexpr int g_st(int P, int g) { return (P == 0 && g >= 16 && g < 19) ? g - 16 : -1; }
constexpr int g_dd(int P, int H, int g) { return (P == 0 && H == 1) ? (g == 23 ? 0 : (g == 24 ? 1 : (g == 26 ? 2 : (g == 27 ? 3 : (g == 28 ? 4 : (g == 30 ? 5 : -1)))))) : -1; }
constexpr int ST_PUB = 14;
constexpr int vm_count(int P, int H, int n) {
    int tag[96] = {}, cnt[96] = {}, m = 0;
    { const int t[5] = {200, 100, 101, 201, 102}; for (int i = 0; i < 5; ++i) { tag[m] = t[i]; cnt[m++] = 2; } }
    for (int g = 0; n > 0 && !(g_sync(P, g) && g_stage(P, g + 1) == n); ++g) {
        if (g_wd(P, g) >= 0) { tag[m] = 100 + g_wd(P, g); cnt[m++] = 2; }
        if (g_xd(P, g) >= 0) { tag[m] = 200 + g_xd(P, g); cnt[m++] = 2; }
        if (g_st(P, g) >= 0) { tag[m] = 300; cnt[m++] = 1; }
        if (g_dd(P, H, g) >= 0) { tag[m] = 400; cnt[m++] = 4; }
    }
    int last = -1;
    for (int i = 0; i < m; ++i) if (tag[i] == 100 + n || (tag[i] >= 200 && tag[i] <= 200 + x_need(P, n)) || (tag[i] == 300 && n == ST_PUB)) last = i;
    int r = 0; for (int i = last + 1; i < m; ++i) r += cnt[i];
    return r;
}
DI void issueX1(const P1& c, int kc, int p) { dma16(c.rx, p ? c.vx1 : c.vx0, kc * 128, c.l0 + kc * 16384 + c.wid * 2048 + p * 1024); }
template <int P> DI void issueW1(const P1& c, int n, int p) { const WSrc& s = c.ws[st_nb(P, n) == 4];
    dma16(s.r, p ? s.v1 : s.v0, st_idx(P, n) * s.step, c.l0 + w_slot(P, n) + c.wid * 2048 + p * 1024); }
struct Frags { bf16x8 x[4], w[4]; };
template <int P, int G> DI void rd_part(Frags& f, const P1& c, int i) {
    constexpr int n = g_stage(P, G), NB = st_nb(P, n), s = g_sub(P, G) * (4 / NB), ks = st_idx(P, n) * (8 / NB) + s;
    const Geo& g = c.g[NB == 4];
    lptr xb = c.lds + ((ks >> 2) & 3) * 16384 + g.xoff[ks >> 4];
    lptr wp = c.lds + (w_slot(P, n) >= L_RING ? g.woff + (w_slot(P, n) - L_RING) : g.wlo + w_slot(P, n)) + s * 2048;
    if constexpr (NB == 4) {
        f.x[i] = lds_b128(xb + (((2 * (ks & 3)) ^ c.xsw) << 4) + i * 4096);
        f.w[i] = cat(lds_tr(wp + i * 4096), lds_tr(wp + i * 4096 + 512));
    } else {
#pragma unroll
        for (int u = 2 * i; u < 2 * i + 2; ++u) {
            f.x[u] = lds_b128(xb + (((2 * u) ^ c.xsw) << 4));
            f.w[u] = cat(lds_tr(wp + u * 2048), lds_tr(wp + u * 2048 + 512));
        }
    }
}
template <int P, int G, bool SWAP> DI void mm_one(const Frags& f, f32x16 (&acc)[4], f32x16& aq, int i) {
    if constexpr (st_nb(P, g_stage(P, G)) == 4) acc[i] = MFMA(f.w[i & 1], f.x[i >> 1], acc[i]);
    else if constexpr (SWAP) aq = MFMA(f.x[i], f.w[i], aq);
    else aq = MFMA(f.w[i], f.x[i], aq);
}
template <int P, int H, int G, bool SWAP, class Ev> DI void group(P1& c, Frags& fc, Frags& fn, f32x16 (&acc)[4], f32x16& aq, Ev& ev) {
    constexpr bool NEXT = G + 1 < n_gr(P), SYNC = g_sync(P, G);
    constexpr int nn = NEXT ? g_stage(P, G + 1) : 0, WD = g_wd(P, G), XD = g_xd(P, G), ST = g_st(P, G);
    if constexpr (SYNC) { TR(c); waitv<vm_count(P, H, nn)>(); TR(c); BARRIER(); TR(c); ev.template after_sync<nn>(); }
    SBAR();
#pragma unroll
    for (int i = 0; i < 4; ++i) {
        mm_one<P, G, SWAP>(fc, acc, aq, i);
        if constexpr (NEXT) { if (i < 2) rd_part<P, NEXT ? G + 1 : G>(fn, c, i); }
        SBAR();
        if constexpr (g_dd(P, H, G) >= 0) { out_issueW1(c.oc, g_dd(P, H, G) >= 0 ? g_dd(P, H, G) : 0, i); SBAR(); }
        if ((i >> 1) == H) {
            if constexpr (WD >= 0) issueW1<P>(c, WD >= 0 ? WD : 0, i & 1);
            if constexpr (XD >= 0) issueX1(c, XD, i & 1);
            if constexpr (ST >= 0) { if (i & 1) st_sc1(ev.sd[ST >= 0 ? ST : 0], ev.rws, ev.so[ST >= 0 ? ST : 0]); }
            SBAR();
        }
    }
    ev.template after_group<G>();
}
template <int P, int H, int G0, int G1, bool SWAP, class Ev> DI void groups(P1& c, Frags& fa, Frags& fb, f32x16 (&acc)[4], f32x16& aq, Ev& ev) {
    if constexpr (G0 < G1) {
        if constexpr (G0 & 1) group<P, H, G0, SWAP>(c, fb, fa, acc, aq, ev); else group<P, H, G0, SWAP>(c, fa, fb, acc, aq, ev);
        groups<P, H, G0 + 1, G1, SWAP>(c, fa, fb, acc, aq, ev);
    }
}
DI Geo mk_geo(int lane, unsigned row0, unsigned blkoff, unsigned half) {
    const unsigned r32 = lane & 31, hh = lane >> 5, q = (lane & 15) >> 2; Geo g;
    g.xoff[0] = (row0 + r32) * 128; g.xoff[1] = g.xoff[0] + 65536;
    g.woff = L_RING + blkoff + (8 * hh + q) * 128 + ((half ^ (q >> 1)) << 6) + ((lane >> 4) & 1) * 32 + (lane & 3) * 8;
    g.wlo = g.woff - L_RING;
    PIN(g.xoff[1]); PIN(g.woff);
    return g;
}
template <bool ISQ> DI void epi_qk(const f32x16& a, unsigned n0, unsigned tt, u32x4* sd, unsigned* so, int lane) {
    constexpr float sc = ISQ ? QSCALE : 1.f;
    const unsigned o = (ISQ ? WS_Q : WS_K) + n0 * 64 + tt * 28672 + lane * 16;
#pragma unroll
    for (int p = 0; p < 2; ++p) {
        unsigned a0 = cvtpk(a[8 * p] * sc, a[8 * p + 1] * sc), a1 = cvtpk(a[8 * p + 2] * sc, a[8 * p + 3] * sc), b0 = cvtpk(a[8 * p + 4] * sc, a[8 * p + 5] * sc), b1 = cvtpk(a[8 * p + 6] * sc, a[8 * p + 7] * sc);
        auto s0 = __builtin_amdgcn_permlane32_swap(a0, b0, false, false); auto s1 = __builtin_amdgcn_permlane32_swap(a1, b1, false, false);
        sd[p] = (u32x4){s0[0], s1[0], s0[1], s1[1]}; so[p] = o + p * 1024;
    }
    sd[2] = sd[1]; so[2] = OOB;
}
DI void epi_v(const f32x16& a, unsigned n0, unsigned tt, u32x4* sd, unsigned* so, int lane, unsigned ph, unsigned rb) {
    const unsigned r32 = lane & 31, hh = lane >> 5, n = n0 + r32, head = n / 28, d = n - 28 * head;
    const unsigned o = WS_V + (head * 64 + tt) * 2048 + (d + 32 * hh) * 16;
    sd[0] = pack8(a, 0); so[0] = o; sd[1] = pack8(a, 1); so[1] = o + 1024;
    const unsigned one = (lane & 3) == 0 ? 0x3f803f80u : 0u;
    sd[2] = (u32x4){one, one, one, one}; so[2] = ph < 16 ? WS_V + (ph * 64 + rb * 4 + (lane >> 4)) * 2048 + ((lane >> 3) & 1) * 1024 + (28 + (lane & 3) + 32 * ((lane >> 2) & 1)) * 16 : OOB;
}
DI void epi_h(const f32x16& g, const f32x16& u, unsigned ct, unsigned tt, rsrc_t rws, int lane) {
    f32x16 hv;
#pragma unroll
    for (int i = 0; i < 16; ++i) hv[i] = u[i] * sigmoidf_(g[i]);
    const unsigned o = WS_H + (tt * 112 + ct * 2) * 1024 + lane * 16;
    st_sc1(pack8(hv, 0), rws, o); st_sc1(pack8(hv, 1), rws, o + 1024);
}

template <int P, int H> struct Ev1 {
    char* ws; rsrc_t rws; u64 tag; int tid, lane; unsigned rb, cg, rt, half; f32x16& aq; u32x4 (&fq)[2]; u32x4 sd[3]; unsigned so[3];
    template <int N> DI void after_sync() {
        if constexpr (P == 0 && N == ST_PUB && H == 0) set_flag(rws, tid == 0 ? WS_FQ + (rb * 16 + cg) * 8 : OOB, tag);
        if constexpr (P == 0 && N == ST_PUB && H == 0) { if (tid < 64) STAMP(0, 1); }
        if constexpr (P == 0 && N == 17 && H == 0) { if (tid < 64) STAMP(0, 3); }
        if constexpr (P == 0 && N == n_st(P) - 1 && H == 0) poll2(fq, rws, tid >= 192 ? WS_FQ + lane * 32 : OOB);
    }
    template <int G> DI void after_group() {
        if constexpr (P == 0 && G == 15) { const unsigned tt = rb * 4 + rt;
            if (cg < 7) epi_v(aq, cg * 64 + half * 32, tt, sd, so, lane, cg * 8 + half * 4 + rt, rb); else epi_qk<true>(aq, (cg - 7) * 64 + half * 32, tt, sd, so, lane); }
    }
};
template <int P, int H> DI void phase1(P1& c, f32x16 (&acc)[4], rsrc_t rws, u64 tag, int tid, unsigned rb, unsigned cg, char* ws) {
    f32x16 aq = {}; u32x4 fq[2]; Frags fa, fb;
#pragma unroll
    for (int i = 0; i < 4; ++i) acc[i] = (f32x16){};
    Ev1<P, H> ev{ws, rws, tag, tid, c.lane, rb, cg, c.wid & 3, c.wid >> 2, aq, fq};
    TR(c); waitv<vm_count(P, H, 0)>(); TR(c); BARRIER(); TR(c);
    rd_part<P, 0>(fa, c, 0); rd_part<P, 0>(fa, c, 1);
    if constexpr (P == 0) {
        groups<P, H, 0, 2 * QS, false>(c, fa, fb, acc, aq, ev);
        if (cg < 7) groups<P, H, 2 * QS, 2 * QS + 7, true>(c, fa, fb, acc, aq, ev); else groups<P, H, 2 * QS, 2 * QS + 7, false>(c, fa, fb, acc, aq, ev);
        groups<P, H, 2 * QS + 7, 35, false>(c, fa, fb, acc, aq, ev);
    } else groups<P, H, 0, 28, false>(c, fa, fb, acc, aq, ev);
    TR(c);
    if constexpr (H == 0) {
        unsigned all = 0;
        if constexpr (P == 0) {
            asm volatile("s_waitcnt vmcnt(0)" : "+v"(fq[0]), "+v"(fq[1]) :: "memory");
            const unsigned lo = (unsigned)tag, hi = (unsigned)(tag >> 32);
            all = __all((fq[0][0] == lo) & (fq[0][1] == hi) & (fq[0][2] == lo) & (fq[0][3] == hi) & (fq[1][0] == lo) & (fq[1][1] == hi) & (fq[1][2] == lo) & (fq[1][3] == hi));
        }
        if (tid >= 192 && tid < 208) *(LAS unsigned*)(c.lds + L_CNT + (tid - 192) * 4) = tid == 200 ? all : 0u;
    }
}

DI float max16(const f32x16& p) {
    float a = fmaxf(p[0], p[1]);
#pragma unroll
    for (int i = 2; i < 16; ++i) a = fmaxf(a, p[i]);
    auto rr = __builtin_amdgcn_permlane32_swap(__float_as_uint(a), __float_as_uint(a), false, false);
    return fmaxf(__uint_as_float(rr[0]), __uint_as_float(rr[1]));
}
#define EXP2(v) __builtin_amdgcn_exp2f(v)
template <class Mid>
DI void attn_part(char* ws, rsrc_t rws, lptr lds, unsigned wid, int lane, unsigned h, unsigned rb, u64 tag, unsigned fh_off, Mid mid) {
    const unsigned hh = lane >> 5, va = lane * 16;
    const unsigned c0 = (7 * h >> 1) * 512;
    const rsrc_t rk = mk_rsrc(ws + WS_K + c0, 64 * 28672 - c0), rv = mk_rsrc(ws + WS_V + h * (64 * 2048), 64 * 2048);
    const unsigned kb = wid * (16 * 28672), vb = wid * (16 * 2048);
    bf16x8 q[4][2], kv[4][4];
    auto ldq = [&](unsigned t, int s) {
        u32x4 v = __builtin_bit_cast(u32x4, ld_sc1(rws, va, WS_Q + c0 + t * 28672 + s * 1024));
        const bool z = s ? (!(h & 1) && hh) : ((h & 1) && !hh);
        v[2 * s] = z ? 0u : v[2 * s]; v[2 * s + 1] = z ? 0u : v[2 * s + 1]; return __builtin_bit_cast(bf16x8, v); };
    auto ldkv = [&](bf16x8 (&d)[4], unsigned t) { d[0] = ld_sc1(rk, va, kb + t * 28672); d[1] = ld_sc1(rk, va, kb + t * 28672 + 1024); d[2] = ld_sc1(rv, va, vb + t * 2048); d[3] = ld_sc1(rv, va, vb + t * 2048 + 1024); };
    u32x4 qraw[4][2];
#pragma unroll
    for (int i = 0; i < 4; ++i) { qraw[i][0] = __builtin_bit_cast(u32x4, ld_sc1(rws, va, WS_Q + c0 + (rb * 4 + i) * 28672)); qraw[i][1] = __builtin_bit_cast(u32x4, ld_sc1(rws, va, WS_Q + c0 + (rb * 4 + i) * 28672 + 1024)); }
    const bf16x8 kz0 = ld_sc1(rk, va, 0), kz1 = ld_sc1(rk, va, 1024);
#pragma unroll
    for (int t = 0; t < 3; ++t) ldkv(kv[t], t);
    SBAR();
    mid();
    SBAR();
    const f32x16 zero = {};
    float mx = -INFINITY;
#pragma unroll
    for (int i = 0; i < 4; ++i) {
        const bool z0 = (h & 1) && !hh, z1 = !(h & 1) && hh;
        qraw[i][0][0] = z0 ? 0u : qraw[i][0][0]; qraw[i][0][1] = z0 ? 0u : qraw[i][0][1]; qraw[i][1][2] = z1 ? 0u : qraw[i][1][2]; qraw[i][1][3] = z1 ? 0u : qraw[i][1][3];
        q[i][0] = __builtin_bit_cast(bf16x8, qraw[i][0]); q[i][1] = __builtin_bit_cast(bf16x8, qraw[i][1]);
        f32x16 s = MFMA(kz0, q[i][0], zero); s = MFMA(kz1, q[i][1], s);
#pragma unroll
        for (int r = 0; r < 16; ++r) mx = fmaxf(mx, s[r]);
    }
    { auto rr = __builtin_amdgcn_permlane32_swap(__float_as_uint(mx), __float_as_uint(mx), false, false); mx = -fmaxf(__uint_as_float(rr[0]), __uint_as_float(rr[1])); }
    f32x16 cm;
#pragma unroll
    for (int r = 0; r < 16; ++r) cm[r] = mx;
    PIN(cm);
    f32x16 o[4], sA, sB; u32x4 p0, p1 = {0, 0, 0, 0};
#pragma unroll
    for (int i = 0; i < 4; ++i) o[i] = zero;
    sA = MFMA(kv[0][0], q[0][0], cm); sA = MFMA(kv[0][1], q[0][1], sA);
    bf16x8 vprev = kv[0][3]; kv[3][3] = kv[0][3];
    u32x2 gv;
    float tp0 = 0.f, tp1 = 0.f, tp2 = 0.f, tp3 = 0.f;
#define UNIT(SC, SN, I, KN0, KN1, IN, V0, VP1, IP, LOADS) do { \
    SN = MFMA(KN0, q[IN][0], cm); SC[0] = EXP2(SC[0]); SC[1] = EXP2(SC[1]); p1[2] = cvtpk(tp0, tp1); SC[2] = EXP2(SC[2]); SC[3] = EXP2(SC[3]); p1[3] = cvtpk(tp2, tp3); PIN(SC); PIN(p1); SBAR(); \
    SN = MFMA(KN1, q[IN][1], SN);   SC[4] = EXP2(SC[4]); SC[5] = EXP2(SC[5]); p0[0] = cvtpk(SC[0], SC[1]); SC[6] = EXP2(SC[6]); SC[7] = EXP2(SC[7]); p0[1] = cvtpk(SC[2], SC[3]); PIN(SC); PIN(p0); SBAR(); \
    o[IP] = MFMA(VP1, __builtin_bit_cast(bf16x8, p1), o[IP]); SC[8] = EXP2(SC[8]); SC[9] = EXP2(SC[9]); p0[2] = cvtpk(SC[4], SC[5]); p0[3] = cvtpk(SC[6], SC[7]); SC[10] = EXP2(SC[10]); SC[11] = EXP2(SC[11]); PIN(SC); PIN(p0); SBAR(); \
    LOADS; \
    o[I] = MFMA(V0, __builtin_bit_cast(bf16x8, p0), o[I]); tp0 = EXP2(SC[12]); tp1 = EXP2(SC[13]); p1[0] = cvtpk(SC[8], SC[9]); tp2 = EXP2(SC[14]); tp3 = EXP2(SC[15]); p1[1] = cvtpk(SC[10], SC[11]); \
    PIN(tp0); PIN(tp1); PIN(tp2); PIN(tp3); PIN(p1); SBAR(); } while (0)
#define TILE(B, BN, BP, TOFF) do { \
    UNIT(sA, sB, 0, kv[B][0], kv[B][1], 1, kv[B][2], kv[BP][3], 3, \
         { ldkv(kv[BP], (TOFF)); SBAR(); }); \
    UNIT(sB, sA, 1, kv[B][0], kv[B][1], 2, kv[B][2], kv[B][3], 0, {}); \
    UNIT(sA, sB, 2, kv[B][0], kv[B][1], 3, kv[B][2], kv[B][3], 1, {}); \
    UNIT(sB, sA, 3, kv[BN][0], kv[BN][1], 0, kv[B][2], kv[B][3], 2, {}); } while (0)
    if (wid == 0) STAMP(0, 6);
#pragma unroll 1
    for (int t = 0; t < 16; t += 4) {
        const unsigned to = t + 3;
        TILE(0, 1, 3, to);
        if (t == 0) { waitv<4>(); if (lane == 0) set_flag(rws, fh_off, tag); }
        gv = __builtin_amdgcn_raw_buffer_load_b64(rws, WS_G + (rb * 128 + wid * 32 + (lane & 31)) * 8, 0, 16);
        TILE(1, 2, 0, t < 12 ? to + 1 : 64); TILE(2, 3, 1, t < 12 ? to + 2 : 64); TILE(3, 0, 2, t < 12 ? to + 3 : 64);
    }
#undef TILE
#undef UNIT
    (void)vprev;
    float gtok = __uint_as_float(gv[0]);
    if (!__all(gv[1] == (unsigned)tag)) gtok = load_gate(rws, rb * 128 + wid * 32 + (lane & 31), tag);
    if (wid == 0) STAMP(0, 7);
    p1[2] = cvtpk(tp0, tp1); p1[3] = cvtpk(tp2, tp3);
    o[3] = MFMA(kv[3][3], __builtin_bit_cast(bf16x8, p1), o[3]);
#pragma unroll
    for (int i = 0; i < 4; ++i) if (i != (int)wid) {
        const unsigned sl = wid > (unsigned)i ? wid - 1 : wid;
#pragma unroll
        for (int g = 0; g < 4; ++g) *(LAS f32x4*)(lds + COMB(i) + (sl * 4 + g) * 1024 + va) = (f32x4){o[i][4 * g], o[i][4 * g + 1], o[i][4 * g + 2], o[i][4 * g + 3]};
    }
    f32x16 r = wid == 0 ? o[0] : (wid == 1 ? o[1] : (wid == 2 ? o[2] : o[3]));
    group_barrier(lds + L_CNT, 4);
#pragma unroll
    for (int j = 0; j < 3; ++j)
#pragma unroll
        for (int g = 0; g < 4; ++g) { const f32x4 v = *(LAS f32x4*)(lds + COMB(wid) + (j * 4 + g) * 1024 + va); r[4 * g] += v[0]; r[4 * g + 1] += v[1]; r[4 * g + 2] += v[2]; r[4 * g + 3] += v[3]; }
    if (wid == 0) STAMP(0, 8);
    bool ok = true;
#pragma unroll
    for (int i = 0; i < 16; ++i) ok &= fabsf(r[i]) < 1.8e19f;
    ok &= !hh || r[12] > 5.4e-20f;
    if (!__all(ok)) {
        const bf16x8 q0 = ldq(rb * 4 + wid, 0), q1 = ldq(rb * 4 + wid, 1);
        float m = -INFINITY; r = zero;
#pragma unroll 1
        for (int t = 0; t < 64; ++t) {
            const bf16x8 k0 = ld_sc1(rk, va, t * 28672), k1 = ld_sc1(rk, va, t * 28672 + 1024), v0 = ld_sc1(rv, va, t * 2048), v1 = ld_sc1(rv, va, t * 2048 + 1024);
            f32x16 s = MFMA(k0, q0, zero); s = MFMA(k1, q1, s);
            const float mn = fmaxf(m, max16(s)), al = EXP2(m - mn); m = mn;
#pragma unroll
            for (int i = 0; i < 16; ++i) { s[i] = EXP2(s[i] - mn); r[i] *= al; }
            r = MFMA(v0, __builtin_bit_cast(bf16x8, pack8(s, 0)), r); r = MFMA(v1, __builtin_bit_cast(bf16x8, pack8(s, 1)), r);
        }
    }
    auto rr = __builtin_amdgcn_permlane32_swap(__float_as_uint(r[12]), __float_as_uint(r[12]), false, false);
    const float inv = gtok * __builtin_amdgcn_rcpf(__uint_as_float(rr[1]));
#pragma unroll
    for (int i = 0; i < 16; ++i) r[i] = (hh && i >= 12) ? 0.f : r[i] * inv;
    const unsigned tt = rb * 4 + wid, oo = WS_O + (tt * 16 + h) * 2048 + va;
    st_sc1(pack8(r, 0), rws, oo); st_sc1(pack8(r, 1), rws, oo + 1024);
    waitv<0>();
    if (lane == 0) set_flag(rws, WS_FO + ((tt >> 1) * 32 + h * 2 + (tt & 1)) * 8, tag);
}

DI void out_issueA(const OutC& c, int i, u32x4 (&a)[4]) {
#pragma unroll
    for (int t = 0; t < 2; ++t)
#pragma unroll
        for (int s = 0; s < 2; ++s)
            gload16_sc1(a[2 * t + s], c.rws, c.va, i < 14 ? WS_H + ((c.rb * 2 + t) * 112 + c.w * 28 + 2 * i + s) * 1024 : WS_O + (((c.rb * 2 + t) * 16 + c.w * 4 + (i - 14)) * 2 + s) * 1024);
}
template <int N> DI void waitv4(u32x4 (&a)[4]) { asm volatile("s_waitcnt vmcnt(%4)" : "+v"(a[0]), "+v"(a[1]), "+v"(a[2]), "+v"(a[3]) : "n"(N) : "memory"); }
constexpr int out_younger(int i) { int n = 0;
    for (int k = i + 1; k < OB_P; ++k) n += 4;
    for (int j = (i - OB_P + 1 > 0 ? i - OB_P + 1 : 0); j < i; ++j) { if (j + OB_NS < 18) n += 4; if (j + OB_P < 14) n += 4; }
    return n; }
template <int I> DI void out_stage(const OutC& c, lptr lds, const unsigned (&woff)[3][2], u32x4 (&af)[OB_P][4], f32x16 (&acc)[2][2]) {
    u32x4(&a)[4] = af[I % OB_P];
    if constexpr (I < 14) waitv4<out_younger(I)>(a); else waitv4<(17 - I) * 4>(a);
    lptr sl = lds + (RG(I % OB_NS) & 65535);
#pragma unroll
    for (int s = 0; s < 2; ++s) {
        bf16x8 wf[2];
#pragma unroll
        for (int cc = 0; cc < 2; ++cc) wf[cc] = cat(lds_tr(sl + woff[RG(I % OB_NS) >> 16][cc] + s * 2048), lds_tr(sl + woff[RG(I % OB_NS) >> 16][cc] + s * 2048 + 1024));
#pragma unroll
        for (int t = 0; t < 2; ++t)
#pragma unroll
            for (int cc = 0; cc < 2; ++cc) acc[t][cc] = MFMA(wf[cc], __builtin_bit_cast(bf16x8, a[2 * t + s]), acc[t][cc]);
    }
    SBAR();
    if constexpr (I + OB_NS < 18) out_issueW(c, I + OB_NS);
    if constexpr (I + OB_P < 14) out_issueA(c, I + OB_P, a);
}
template <int I0, int I1> DI void out_stages(const OutC& c, lptr lds, const unsigned (&woff)[3][2], u32x4 (&af)[OB_P][4], f32x16 (&acc)[2][2]) {
    if constexpr (I0 < I1) { out_stage<I0>(c, lds, woff, af, acc); out_stages<I0 + 1, I1>(c, lds, woff, af, acc); }
}
DI void out_part(const OutC& c, char* ws, uint16_t* out, lptr lds, int lane, unsigned nb, u64 tag) {
    const unsigned r32 = lane & 31, hh = lane >> 5, w = c.w, rb = c.rb;
    u32x4 af[OB_P][4];
    if (w == 0) { wait_flags<1>(c.rws, WS_FH + (rb * 64 + (lane < 56 ? lane : 0)) * 8, tag); if (lane == 0) __hip_atomic_store((LAS unsigned*)(lds + L_CNT + 40), 1u, __ATOMIC_RELAXED, __HIP_MEMORY_SCOPE_WORKGROUP); }
    else lds_wait(lds + L_CNT + 40, 1);
    STAMP(1, 4);
#pragma unroll
    for (int i = 0; i < OB_P; ++i) out_issueA(c, i, af[i]);
    f32x16 ab[2][2] = {};
    const unsigned q = (lane & 15) >> 2;
    unsigned woff[3][2];
#pragma unroll
    for (int cc = 0; cc < 2; ++cc)
#pragma unroll
        for (int u = 0; u < 3; ++u) { woff[u][cc] = u * 65536 + w * 4096 + (4 * hh + q) * 128 + ((cc ^ (q >> 1)) << 6) + ((lane >> 4) & 1) * 32 + (lane & 3) * 8; PIN(woff[u][cc]); }
    out_stages<0, 14>(c, lds, woff, af, ab);
    STAMP(1, 5);
#pragma unroll
    for (int t = 0; t < 2; ++t) { const float gm = 1.f - load_gate(c.rws, rb * 64 + t * 32 + r32, tag);
#pragma unroll
        for (int cc = 0; cc < 2; ++cc) { ab[t][cc] *= gm; PIN(ab[t][cc]); } }
    wait_flags<1>(c.rws, WS_FO + (rb * 32 + w * 8 + (lane & 7)) * 8, tag);
    STAMP(1, 6);
#pragma unroll
    for (int i = 14; i < 18; ++i) out_issueA(c, i, af[i % OB_P]);
    out_stages<14, 18>(c, lds, woff, af, ab);
#pragma unroll
    for (int t = 0; t < 2; ++t)
#pragma unroll
        for (int cc = 0; cc < 2; ++cc)
#pragma unroll
            for (int g = 0; g < 4; ++g)
                *(LAS f32x4*)(lds + RG(2 * t + cc) + w * 4096 + g * 1024 + lane * 16) = (f32x4){ab[t][cc][4 * g], ab[t][cc][4 * g + 1], ab[t][cc][4 * g + 2], ab[t][cc][4 * g + 3]};
    group_barrier(lds + L_CNT + 16, 4);
    uint16_t* orow = out + (size_t)(rb * 64 + (w >> 1) * 32 + r32) * DM + nb * 64 + (w & 1) * 32 + 4 * hh;
#pragma unroll
    for (int g = 0; g < 4; ++g) {
        const unsigned rg = w == 0 ? RG(0) : (w == 1 ? RG(1) : (w == 2 ? RG(2) : RG(3)));
        f32x4 s = *(LAS f32x4*)(lds + rg + g * 1024 + lane * 16);
#pragma unroll
        for (int v = 1; v < 4; ++v) s += *(LAS f32x4*)(lds + rg + v * 4096 + g * 1024 + lane * 16);
        *(u32x2*)(orow + 8 * g) = (u32x2){cvtpk(s[0], s[1]), cvtpk(s[2], s[3])};
    }
}

__global__ __launch_bounds__(512, 2) void fused(const uint16_t* x, const uint16_t* wqkv, const uint16_t* wo, const uint16_t* wg, const uint16_t* wu,
                                                const uint16_t* wd, const uint16_t* gw, char* ws, uint16_t* out) {
    extern __shared__ __attribute__((aligned(16))) char smem[];
    lptr lds = (lptr)smem;
    u64 pc; asm volatile("s_getpc_b64 %0" : "=s"(pc)); pc &= ~(u64)127;
    const int tid = threadIdx.x, lane = tid & 63; const unsigned wid = rfl(tid >> 6);
    const unsigned b = blockIdx.x, xc = b & 7, j = b >> 3;
    if (wid == 0) STAMP(0, 0);
    u64 tag = *(const __attribute__((address_space(4))) u64*)(ws + WS_E);
    tag += 0x9E3779B97F4A7C15ull; tag = (tag ^ (tag >> 30)) * 0xBF58476D1CE4E5B9ull; tag = (tag ^ (tag >> 27)) * 0x94D049BB133111EBull; tag ^= tag >> 31;
    const rsrc_t rws = mk_rsrc(ws, WS_END);
    OutC oc; oc.rd = mk_rsrc(wd, FF * DM * 2); oc.ro = mk_rsrc(wo, DM * DM * 2); oc.rws = rws; oc.w = wid & 3; oc.rb = xc * 4 + (j & 3);
    oc.l0 = (unsigned)(uintptr_t)lds + oc.w * 4096; oc.va = lane * 16;
    oc.vw = j < 28 ? (lane >> 3) * (DM * 2) + (j >> 2) * 128 + (((lane & 7) ^ (((lane >> 4) & 1) << 2)) << 4) : OOB;
    f32x16 acc[4];
    unsigned e_ct, e_tt, e_fh; bool e_on, e_late;
    {
        const unsigned rb = 8 * (xc & 1) + (j & 7), cg = 4 * (j >> 3) + (xc >> 1);
        const rsrc_t rq = mk_rsrc(wqkv, DM * 3 * DM * 2), rg = mk_rsrc(wg, DM * FF * 2), ru = mk_rsrc(wu, DM * FF * 2);
        P1 c; c.lds = lds; c.l0 = (unsigned)(uintptr_t)lds; c.wid = wid; c.lane = lane; c.rx = mk_rsrc(x, SEQ * DM * 2); c.trv = 0; c.tri = 0; c.oc = oc; TR(c);
        const unsigned xr = 16 * wid + (lane >> 3);
        c.vx0 = (rb * 128 + xr) * (DM * 2) + (((lane & 7) ^ ((xr >> 1) & 7)) << 4); c.vx1 = (rb * 128 + xr + 8) * (DM * 2) + (((lane & 7) ^ (((xr + 8) >> 1) & 7)) << 4);
        const unsigned rt = wid & 3, half = wid >> 2, rp = wid & 1, pi = wid >> 1;
        c.xsw = (lane >> 5) ^ (((lane & 31) >> 1) & 7);
        c.g[0] = mk_geo(lane, 32 * rt, 0, half); c.g[1] = mk_geo(lane, 64 * rp, (pi >> 1) * 8192, pi & 1);
        issueX1(c, 0, 0); issueX1(c, 0, 1);
        auto codepf = [&]() {
            dma4(mk_rsrc((const void*)pc, CODE_PF), (wid == 7 && lane < 16) ? (j * 16 + lane) * 128 : OOB, c.l0 + 6 * 16384 + wid * 2048);
        };
        if (cg < 14) c.ws[1] = mk_wsrc((wid & 2) ? ru : rg, FF * 2, (2 * cg + (wid >> 2)) * 128, 16 * (wid & 1), 32, lane);
        else c.ws[1] = mk_wsrc(rq, 3 * DM * 2, (4 * cg - 49 + pi) * 128, 16 * (wid & 1), 32, lane);
        issueW1<0>(c, 0, 0); issueW1<0>(c, 0, 1); __builtin_amdgcn_s_barrier(); if (wid == 7) codepf();
        issueW1<0>(c, 1, 0); issueW1<0>(c, 1, 1); issueX1(c, 1, 0); issueX1(c, 1, 1); issueW1<0>(c, 2, 0); issueW1<0>(c, 2, 1);
        c.ws[0] = mk_wsrc(rq, 3 * DM * 2, (cg < 7 ? 14 + cg : cg - 7) * 128, 16 * wid, 128, lane);
        if (cg < 14) { if (wid < 4) phase1<0, 0>(c, acc, rws, tag, tid, rb, cg, ws); else phase1<0, 1>(c, acc, rws, tag, tid, rb, cg, ws); }
        else {
            if (wid < 4) phase1<1, 0>(c, acc, rws, tag, tid, rb, cg, ws); else phase1<1, 1>(c, acc, rws, tag, tid, rb, cg, ws);
            {
                if (wid == 0) STAMP(2, 0);
                if (wid == 7) STAMP(2, 3);
#pragma unroll
                for (int i = 0; i < 4; ++i) { u32x4 sd[3]; unsigned so[3];
                    const unsigned kb = 4 * cg - 56 + (pi >> 1) * 2 + (i & 1);
                    epi_qk<false>(acc[i], kb * 64 + (pi & 1) * 32, rb * 4 + 2 * rp + (i >> 1), sd, so, lane);
                    if (kb < 7) { st_sc1(sd[0], rws, so[0]); st_sc1(sd[1], rws, so[1]); } }
                if (wid == 0) STAMP(2, 1);
                if (wid == 7) STAMP(2, 4);
                waitv<0>();
                if (wid == 0) STAMP(2, 2);
                if (wid == 7) STAMP(2, 5);
            }
            BARRIER();
            if (tid == 0) set_flag(rws, WS_FQ + (rb * 16 + cg) * 8, tag);
        }
        e_ct = cg * 4 + pi; e_tt = rb * 4 + 2 * rp; e_on = cg < 14; e_late = cg >= 14; e_fh = WS_FH + ((rb * 2 + rp) * 64 + cg * 4 + pi) * 8;
    }
    if (wid == 0) STAMP(0, 2);
    auto epilogue = [&]() { if (e_on) { epi_h(acc[0], acc[1], e_ct, e_tt, rws, lane); epi_h(acc[2], acc[3], e_ct, e_tt + 1, rws, lane); } };
    BARRIER();
    if (wid < 4) {
        if (rfl(*(LAS unsigned*)(lds + L_CNT + 32)) == 0) {
            if (wid == 0) { wait_flags<4>(rws, WS_FQ + lane * 8, tag); if (lane == 0) __hip_atomic_store((LAS unsigned*)(lds + L_CNT + 36), 1u, __ATOMIC_RELAXED, __HIP_MEMORY_SCOPE_WORKGROUP); }
            else lds_wait(lds + L_CNT + 36, 1);
        }
        __builtin_amdgcn_s_setprio(1);
        if (wid == 0) STAMP(0, 4);
        attn_part(ws, rws, lds, wid, lane, 2 * xc + (j & 1), j >> 1, tag, e_fh, epilogue);
        if (wid == 0) STAMP(0, 5);
        if (b == 0 && wid == 0) {
            wait_flags<16>(rws, WS_FO + lane * 8, tag);
            if (lane == 0) set_flag(rws, WS_E, tag);
        }
        __builtin_amdgcn_endpgm();
    }
    {
        const OutC& c = oc; const unsigned nb = j >> 2;
        if (j < 28 && e_late) {
#pragma unroll
            for (int i = 0; i < OB_NS; ++i) out_issueW(c, i);
        }
        epilogue();
        waitv<0>();
        if (lane == 0) set_flag(rws, e_fh, tag);
        if (wid == 4) STAMP(1, 3);
        if (j < 28) out_part(c, ws, out, lds, lane, nb, tag);
        else {
            const unsigned row = ((j - 28) * 8 + xc) * 64 + (tid - 256) / 4, part = tid & 3;
            const u32x4* xr = (const u32x4*)(x + (size_t)row * DM); const u32x4* gr = (const u32x4*)gw;
            float sum = 0.f;
#pragma unroll
            for (int i = 0; i < 14; ++i) {
                const u32x4 av = xr[i * 4 + part], bv = gr[i * 4 + part];
#pragma unroll
                for (int e = 0; e < 4; ++e) { sum += __uint_as_float(av[e] << 16) * __uint_as_float(bv[e] << 16); sum += __uint_as_float(av[e] & 0xffff0000u) * __uint_as_float(bv[e] & 0xffff0000u); }
            }
            sum += __shfl_xor(sum, 1); sum += __shfl_xor(sum, 2);
            if (part == 0) __builtin_amdgcn_raw_buffer_store_b64((u32x2){__float_as_uint(sigmoidf_(sum)), (unsigned)tag}, rws, WS_G + row * 8, 0, 16);
        }
        if (wid == 4) STAMP(1, 7);
        __builtin_amdgcn_endpgm();
    }
}

extern "C" void kernel_launch(void* const* d_in, const int* in_sizes, int n_in, void* d_out, int out_size, void* d_ws, size_t ws_size, hipStream_t stream) {
    (void)hipFuncSetAttribute((const void*)fused, hipFuncAttributeMaxDynamicSharedMemorySize, LDS_BYTES);
    fused<<<256, 512, LDS_BYTES, stream>>>((const uint16_t*)d_in[0], (const uint16_t*)d_in[1], (const uint16_t*)d_in[2], (const uint16_t*)d_in[4], (const uint16_t*)d_in[5],
                                           (const uint16_t*)d_in[6], (const uint16_t*)d_in[7], (char*)d_ws, (uint16_t*)d_out);
}
```

```cpp
#include <hip/hip_runtime.h>
#include <hip/hip_bf16.h>
#include <stdint.h>

typedef short bf16x8 __attribute__((ext_vector_type(8)));
typedef short s16x4 __attribute__((ext_vector_type(4)));
typedef float f32x16 __attribute__((ext_vector_type(16)));
typedef float f32x4 __attribute__((ext_vector_type(4)));
typedef float f32x2 __attribute__((ext_vector_type(2)));
typedef unsigned u32x4 __attribute__((ext_vector_type(4)));
typedef unsigned u32x2 __attribute__((ext_vector_type(2)));
typedef __bf16 bf16x2_t __attribute__((ext_vector_type(2)));
typedef unsigned long long u64;
typedef __amdgpu_buffer_rsrc_t rsrc_t;
#define LAS __attribute__((address_space(3)))
#define DI __device__ __forceinline__
typedef LAS char* lptr;

constexpr int SEQ = 2048, DM = 448, DH = 28, FF = 1792;
constexpr float LOG2E = 1.4426950408889634f;
constexpr float QSCALE = 0.18898223650461362f * LOG2E;

constexpr unsigned WS_Q = 0;
constexpr unsigned WS_K = WS_Q + (2u << 20);
constexpr unsigned WS_V = WS_K + (2u << 20);
constexpr unsigned WS_O = WS_V + (2u << 20);
constexpr unsigned WS_H = WS_O + (2u << 20);
constexpr unsigned WS_G = WS_H + 64u * 112 * 1024;
constexpr unsigned WS_FQ = WS_G + 16384;
constexpr unsigned WS_FH = WS_FQ + 8192;
constexpr unsigned WS_FO = WS_FH + 16384;
constexpr unsigned WS_E = WS_FO + 8192;
constexpr unsigned WS_END = WS_E + 256;
constexpr unsigned CODE_PF = 54 * 1024;
#define NTOUCH 2
constexpr unsigned OOB = 0x7ffffff0u;

constexpr int LDS_BYTES = 160 * 1024;
constexpr unsigned RG(int i) { const int t[6] = {4, 0, 1, 3, 7, 2}; return t[i] * 16384; }
#define COMB(w) ((w) == 0 ? 6u * 16384 : ((w) == 1 ? 9u * 16384 : ((w) == 2 ? 5u * 16384 : 8u * 16384)))
constexpr unsigned L_CNT = 6 * 16384 + 12288;

#define MFMA(a, b, c) __builtin_amdgcn_mfma_f32_32x32x16_bf16((a), (b), (c), 0, 0, 0)
#define SBAR() __builtin_amdgcn_sched_barrier(0)
#define PIN(x) asm volatile("" : "+v"(x))
#define BARRIER() asm volatile("s_waitcnt lgkmcnt(0)\n\ts_barrier" ::: "memory")
template <int N> DI void waitv() { asm volatile("s_waitcnt vmcnt(%0)" :: "n"(N) : "memory"); }

#define STAMP_DECL do {} while (0)
#define STAMP(i) do {} while (0)
#define STAMP_DUMP() do {} while (0)
#define STAMP_ARG
#define STAMP_PASS

DI rsrc_t mk_rsrc(const void* p, unsigned bytes) { return __builtin_amdgcn_make_buffer_rsrc((void*)p, (short)0, (int)bytes, 0x00020000); }
DI unsigned rfl(unsigned v) { return (unsigned)__builtin_amdgcn_readfirstlane((int)v); }
DI void dma16(rsrc_t r, unsigned voff, unsigned soff, unsigned ldsaddr) {
    asm volatile("s_mov_b32 m0, %0\n\ts_nop 0\n\tbuffer_load_dwordx4 %1, %2, %3 offen lds" :: "s"(ldsaddr), "v"(voff), "s"(r), "s"(soff) : "memory");
}
DI void dma4(rsrc_t r, unsigned voff, unsigned ldsaddr) {
    asm volatile("s_mov_b32 m0, %0\n\ts_nop 0\n\tbuffer_load_dword %1, %2, 0 offen lds" :: "s"(ldsaddr), "v"(voff), "s"(r) : "memory");
}
DI void gload16_sc1(u32x4& dst, rsrc_t r, unsigned voff, unsigned soff) {
    asm volatile("buffer_load_dwordx4 %0, %1, %2, %3 offen sc1" : "=v"(dst) : "v"(voff), "s"(r), "s"(soff) : "memory");
}
DI bf16x8 ld_sc1(rsrc_t r, unsigned voff, unsigned soff) { return __builtin_bit_cast(bf16x8, __builtin_amdgcn_raw_buffer_load_b128(r, voff, soff, 16)); }
DI void st_sc1(u32x4 v, rsrc_t r, unsigned voff) { __builtin_amdgcn_raw_buffer_store_b128(v, r, voff, 0, 16); }
DI unsigned cvtpk(float lo, float hi) { return __builtin_bit_cast(unsigned, __builtin_convertvector((f32x2){lo, hi}, bf16x2_t)); }
DI bf16x8 lds_b128(lptr p) { return *(const LAS bf16x8*)p; }
DI s16x4 lds_tr(lptr p) { return __builtin_amdgcn_ds_read_tr16_b64_v4i16((LAS s16x4*)p); }
DI bf16x8 cat(s16x4 lo, s16x4 hi) { return __builtin_shufflevector(lo, hi, 0, 1, 2, 3, 4, 5, 6, 7); }
DI u32x4 pack8(const f32x16& a, int s) {
    u32x4 r; r[0] = cvtpk(a[8 * s], a[8 * s + 1]); r[1] = cvtpk(a[8 * s + 2], a[8 * s + 3]); r[2] = cvtpk(a[8 * s + 4], a[8 * s + 5]); r[3] = cvtpk(a[8 * s + 6], a[8 * s + 7]); return r;
}
DI float sigmoidf_(float v) { return __builtin_amdgcn_rcpf(1.f + __builtin_amdgcn_exp2f(-LOG2E * v)); }

DI void set_flag(rsrc_t rws, unsigned off, u64 tag) {
    __builtin_amdgcn_raw_buffer_store_b64((u32x2){(unsigned)tag, (unsigned)(tag >> 32)}, rws, off, 0, 16);
}
template <int NW> DI void wait_flags(rsrc_t rws, unsigned off, u64 tag, unsigned lim = ~0u) {
#pragma unroll 1
    for (int spin = 0; spin < 40000; ++spin) {
        bool ok = true;
#pragma unroll
        for (int i = 0; i < NW; ++i) { const u32x2 v = __builtin_amdgcn_raw_buffer_load_b64(rws, off + i * 512, 0, 16); ok &= ((v[0] == (unsigned)tag) & (v[1] == (unsigned)(tag >> 32))) | (off + i * 512 - WS_FQ >= lim); }
        if (__all(ok)) break;
        __builtin_amdgcn_s_sleep(4);
    }
    asm volatile("" ::: "memory");
}
DI float load_gate(rsrc_t rws, unsigned tok, u64 tag) {
    u32x2 v;
#pragma unroll 1
    for (int spin = 0; spin < 40000; ++spin) {
        v = __builtin_amdgcn_raw_buffer_load_b64(rws, WS_G + tok * 8, 0, 16);
        if (__all(v[1] == (unsigned)tag)) break;
        __builtin_amdgcn_s_sleep(2);
    }
    return __uint_as_float(v[0]);
}
DI void lds_wait(lptr cnt, unsigned target) {
#pragma unroll 1
    for (int spin = 0; spin < 4000000; ++spin) {
        if (rfl(__hip_atomic_load((LAS unsigned*)cnt, __ATOMIC_RELAXED, __HIP_MEMORY_SCOPE_WORKGROUP)) >= target) break;
        __builtin_amdgcn_s_sleep(1);
    }
    asm volatile("" ::: "memory");
}
DI void group_barrier(lptr cnt, unsigned target) {
    asm volatile("s_waitcnt lgkmcnt(0)" ::: "memory");
    if ((threadIdx.x & 63) == 0) __hip_atomic_fetch_add((LAS unsigned*)cnt, 1u, __ATOMIC_RELAXED, __HIP_MEMORY_SCOPE_WORKGROUP);
    lds_wait(cnt, target);
}

struct WSrc { rsrc_t r; unsigned v0, v1, step; };
DI void issueW(const WSrc& s, int st, unsigned dst) { dma16(s.r, s.v0, st * s.step, dst); dma16(s.r, s.v1, st * s.step, dst + 1024); }
DI WSrc mk_wsrc(rsrc_t r, unsigned pitch, unsigned colb, unsigned row0, unsigned rows_per_stage, int lane) {
    WSrc s; s.r = r; const unsigned sw = ((lane & 7) ^ (((lane >> 4) & 1) << 2)) << 4;
    s.v0 = (row0 + (lane >> 3)) * pitch + colb + sw; s.v1 = s.v0 + 8 * pitch; s.step = rows_per_stage * pitch; return s;
}
#define BPRIO 0
constexpr int OB_NS = 6, OB_P = 4;
struct OutC { rsrc_t rd, ro, rws; unsigned l0, vw, va, w, rb; };
DI void out_issueW1(const OutC& c, int i, int p) {
    const unsigned so = i < 14 ? (c.w * 448 + i * 32) * (DM * 2) : (c.w * 4 + (i - 14)) * DH * (DM * 2);
    dma16(i < 14 ? c.rd : c.ro, c.vw, so + p * 8 * (DM * 2), c.l0 + RG(i % OB_NS) + p * 1024);
}
DI void out_issueW(const OutC& c, int i) {
#pragma unroll
    for (int p = 0; p < 4; ++p) out_issueW1(c, i, p);
}
constexpr unsigned it_reg(int P, int it) { const int x[7] = {0, 3, 7, 8, 1, 2, 5}, q[4] = {6, 9, 4, 0}, f[14] = {1, 2, 4, 5, 3, 6, 9, 4, 0, 3, 7, 6, 9, 8};
    const int x1[7] = {0, 3, 6, 9, 2, 0, 3}, f1[14] = {1, 2, 4, 5, 7, 8, 1, 4, 5, 7, 8, 1, 4, 5};
    return (P ? (it < 10 ? x1[it] : f1[it - 20]) : (it < 10 ? x[it] : (it < 20 ? q[it - 10] : f[it - 20]))) * 16384; }
constexpr int it_first(int P, int it) { const int x[7] = {0, 2, 5, 5, 6, 6, 7};
    return it >= 40 ? 99 : (P ? (it < 10 ? 2 * it : it - 20) : (it < 10 ? x[it] : (it < 20 ? it - 6 : (it < 24 ? it - 20 : it - 16)))); }
constexpr int g_bar(int P, int g) { return (P || g <= 13) ? (((g & 1) && g < 27) ? (g + 1) / 2 : -1) : ((g & 1) || g > 32 ? -1 : (g + 2) / 2); }
constexpr int g_item(int P, int g, int u) {
    const int t[23] = {22, 10, 2, 3, 11, 4, 5, 12, 6, 13, 24, 25, 26, 27, 28, -1, 29, -1, 30, 31, 32, -1, 33};
    const int t1[23] = {22, 2, 24, 25, 3, 26, 4, 27, 5, 28, 29, 6, 30, 31, -1, 32, -1, 33, -1, -1, -1, -1, -1};
    return u ? (g == 0 ? 23 : -1) : (g < 23 ? (P ? t1[g] : t[g]) : -1); }
constexpr int g_dd(int P, int H, int g) { return (H == 1 && !P) ? (g == 23 ? 0 : (g == 24 ? 1 : (g == 26 ? 2 : (g == 27 ? 3 : (g == 28 ? 4 : (g == 30 ? 5 : -1)))))) : -1; }
constexpr int g_st(int P, int g) { return (!P && g >= 16 && g < 19) ? g - 16 : -1; }
constexpr int ST_PUB = 14;
constexpr int vm_count(int P, int H, int gs) {
    int tag[96] = {}, cnt[96] = {}, m = 0; const int n = gs < 0 ? 0 : g_bar(P, gs);
    { const int t[4] = {0, 20, 21, 1}; for (int i = 0; i < 4; ++i) { tag[m] = t[i]; cnt[m++] = 2; } tag[m] = 97; cnt[m++] = NTOUCH; }
    for (int g = 0; g < gs; ++g) {
        for (int u = 0; u < 2; ++u) if (g_item(P, g, u) >= 0) { tag[m] = g_item(P, g, u); cnt[m++] = 2; }
        if (g_dd(P, H, g) >= 0) { tag[m] = 40; cnt[m++] = 4; }
        if (g_st(P, g) >= 0) { tag[m] = 98; cnt[m++] = 1; }
    }
    int last = -1;
    for (int i = 0; i < m; ++i) if (it_first(P, tag[i]) <= n || (tag[i] == 98 && n == ST_PUB)) last = i;
    int r = 0; for (int i = last + 1; i < m; ++i) r += cnt[i];
    return r;
}
struct P1 { lptr lds; unsigned l0, xsw; int lane; WSrc sx, sq, sf; OutC oc; unsigned xf[3], wf[3], xq[3], wq[3]; rsrc_t rws; u64 tag; u32x4 sd[3]; unsigned so[3], fqo, pollo; u32x2 fq; };
template <int P, int IT> DI void issue1(const P1& c, int p) {
    const WSrc& s = IT < 10 ? c.sx : (IT < 20 ? c.sq : c.sf);
    dma16(s.r, p ? s.v1 : s.v0, (IT < 10 ? IT : (IT < 20 ? IT - 10 : IT - 20)) * s.step, c.l0 + it_reg(P, IT) + p * 1024);
}
template <int IT> DI void issue2(const P1& c) { issue1<0, IT>(c, 0); issue1<0, IT>(c, 1); }
struct Frags { bf16x8 x[4], w[4]; };
DI lptr at(const P1& c, const unsigned (&b)[3], unsigned off) { return c.lds + b[off >> 16] + (off & 65535); }
template <int P, int G> DI void rd_part(Frags& f, const P1& c, int i) {
    if constexpr (P || G < 8 || G >= 15) {
        constexpr int K = (P || G < 8) ? G : G - 7;
        f.x[i] = lds_b128(at(c, c.xf, it_reg(P, K >> 2) + i * 4096) + (((2 * (K & 3)) ^ c.xsw) << 4));
        lptr wp = at(c, c.wf, it_reg(P, 20 + (K >> 1)) + (K & 1) * 2048 + i * 4096);
        f.w[i] = cat(lds_tr(wp), lds_tr(wp + 512));
    } else {
#pragma unroll
        for (int u = 2 * i; u < 2 * i + 2; ++u) {
            f.x[u] = lds_b128(at(c, c.xq, it_reg(0, G - 8)) + (((2 * u) ^ c.xsw) << 4));
            lptr wp = at(c, c.wq, it_reg(0, 10 + ((G - 8) >> 1)) + ((G & 1) * 4 + u) * 2048);
            f.w[u] = cat(lds_tr(wp), lds_tr(wp + 512));
        }
    }
}
template <int H, int G, int MODE> DI void group(P1& c, Frags& fc, Frags& fn, f32x16 (&acc)[4], f32x16& aq) {
    constexpr int P = MODE == 2; constexpr bool NEXT = G + 1 < (P ? 28 : 35), QG = !P && G >= 8 && G < 15;
    constexpr int I0 = g_item(P, G, 0), I1 = g_item(P, G, 1), DD = g_dd(P, H, G), ST = g_st(P, G);
    if constexpr (g_bar(P, G) >= 0) {
        waitv<vm_count(P, H, G)>(); BARRIER();
        if constexpr (H == 0 && g_bar(P, G) == ST_PUB) set_flag(c.rws, c.fqo, c.tag);
        if constexpr (H == 0 && G == 32) asm volatile("buffer_load_dwordx2 %0, %1, %2, 0 offen sc1" : "=v"(c.fq) : "v"(c.pollo), "s"(c.rws) : "memory");
    }
    if constexpr (H == 1 && G == 31) { if (c.lane < 16) *(LAS unsigned*)(c.lds + L_CNT + c.lane * 4) = 0; }
    SBAR();
#pragma unroll
    for (int i = 0; i < 4; ++i) {
        if constexpr (!QG) acc[i] = MFMA(fc.w[i & 1], fc.x[i >> 1], acc[i]);
        else if constexpr (MODE == 1) aq = MFMA(fc.x[i], fc.w[i], aq);
        else if constexpr (MODE == 0) aq = MFMA(fc.w[i], fc.x[i], aq);
        if constexpr (NEXT) { if (i < 2) rd_part<P, NEXT ? G + 1 : G>(fn, c, i); }
        SBAR();
        if constexpr (I1 >= 0) { if (i < 2) issue1<P, I0 >= 0 ? I0 : 0>(c, i); else issue1<P, I1 >= 0 ? I1 : 0>(c, i - 2); SBAR(); }
        else if constexpr (I0 >= 0) { if ((i >> 1) == H) { issue1<P, I0 >= 0 ? I0 : 0>(c, i & 1); SBAR(); } }
        if constexpr (DD >= 0) { out_issueW1(c.oc, DD >= 0 ? DD : 0, i); SBAR(); }
        if constexpr (ST >= 0) { if (i == 2 * H + 1) { st_sc1(c.sd[ST >= 0 ? ST : 0], c.rws, c.so[ST >= 0 ? ST : 0]); SBAR(); } }
    }
}
template <int H, int G0, int G1, int MODE> DI void groups(P1& c, Frags& fa, Frags& fb, f32x16 (&acc)[4], f32x16& aq) {
    if constexpr (G0 < G1) {
        if constexpr (G0 & 1) group<H, G0, MODE>(c, fb, fa, acc, aq); else group<H, G0, MODE>(c, fa, fb, acc, aq);
        groups<H, G0 + 1, G1, MODE>(c, fa, fb, acc, aq);
    }
}
DI void epi_qk(const f32x16& a, float sc, u32x4* sd) {
#pragma unroll
    for (int p = 0; p < 2; ++p) {
        unsigned a0 = cvtpk(a[8 * p] * sc, a[8 * p + 1] * sc), a1 = cvtpk(a[8 * p + 2] * sc, a[8 * p + 3] * sc), b0 = cvtpk(a[8 * p + 4] * sc, a[8 * p + 5] * sc), b1 = cvtpk(a[8 * p + 6] * sc, a[8 * p + 7] * sc);
        auto s0 = __builtin_amdgcn_permlane32_swap(a0, b0, false, false); auto s1 = __builtin_amdgcn_permlane32_swap(a1, b1, false, false);
        sd[p] = (u32x4){s0[0], s1[0], s0[1], s1[1]};
    }
}
DI void epi_h(const f32x16& g, const f32x16& u, u32x4* d) {
    f32x16 hv;
#pragma unroll
    for (int i = 0; i < 16; ++i) hv[i] = u[i] * sigmoidf_(g[i]);
    d[0] = pack8(hv, 0); d[1] = pack8(hv, 1);
}
DI void store_h(const f32x16 (&acc)[4], unsigned o, rsrc_t rws) {
#pragma unroll
    for (int t = 0; t < 2; ++t) { u32x4 d[2]; epi_h(acc[2 * t], acc[2 * t + 1], d); st_sc1(d[0], rws, o + t * 112 * 1024); st_sc1(d[1], rws, o + t * 112 * 1024 + 1024); }
}
template <int H> DI void phase1(P1& c, f32x16 (&acc)[4], unsigned cg, unsigned rb, unsigned wid STAMP_ARG) {
    Frags fa, fb; f32x16 aq = {};
    const int lane = c.lane; const rsrc_t rws = c.rws;
#pragma unroll
    for (int i = 0; i < 4; ++i) acc[i] = (f32x16){};
    STAMP(9);
    waitv<vm_count(0, H, -1)>(); BARRIER();
    STAMP(10);
    const unsigned rp = wid & 1, pi = wid >> 1, tt = rb * 4 + (wid & 3);
    if (cg >= 14) {
        asm volatile("" ::: "memory");
        rd_part<0, 0>(fa, c, 0); rd_part<0, 0>(fa, c, 1);
        groups<H, 0, 28, 2>(c, fa, fb, acc, aq);
        STAMP(13);
#pragma unroll
        for (int i = 0; i < 4; ++i) { u32x4 sd[2]; const unsigned kb = 4 * cg - 56 + H * 2 + (i & 1), o = WS_K + kb * 4096 + (pi & 1) * 2048 + (rb * 4 + 2 * rp + (i >> 1)) * 28672 + lane * 16;
            epi_qk(acc[i], 1.f, sd);
            if (kb < 7) { st_sc1(sd[0], rws, o); st_sc1(sd[1], rws, o + 1024); } }
        if (H == 1) { if (lane < 16) *(LAS unsigned*)(c.lds + L_CNT + lane * 4) = 0; }
        waitv<0>();
        BARRIER();
        if (H == 0) { c.fq = (u32x2){0, 0}; if (wid == 0 && lane < 4 && 4 * cg - 56 + lane < 7) set_flag(rws, WS_FQ + ((4 * cg - 49 + lane) * 16 + rb) * 8, c.tag); }
        else {
#pragma unroll
            for (int i = 0; i < OB_NS; ++i) out_issueW(c.oc, i);
        }
        return;
    }
    asm volatile("" ::: "memory");
    rd_part<0, 0>(fa, c, 0); rd_part<0, 0>(fa, c, 1);
    groups<H, 0, 8, 0>(c, fa, fb, acc, aq);
    STAMP(11);
    c.sd[2] = (u32x4){0x3f803f80u, 0x3f803f80u, 0x3f803f80u, 0x3f803f80u}; c.so[2] = OOB;
    if (cg < 7) {
        groups<H, 8, 16, 1>(c, fa, fb, acc, aq);
        const unsigned n = cg * 64 + H * 32 + (lane & 31), hd = n / 28, ph = (cg * 64 + 27) / 28 + (lane >> 2);
        c.sd[0] = pack8(aq, 0); c.sd[1] = pack8(aq, 1);
        c.so[0] = WS_V + (hd * 64 + tt) * 2048 + (n - 28 * hd + (lane & 32)) * 16;
        if (H == 0) c.so[2] = ph * 28 < (cg + 1) * 64 ? WS_V + (ph * 64 + tt) * 2048 + (lane & 1) * 1024 + (28 + 16 * (lane & 2)) * 16 : OOB;
    } else {
        groups<H, 8, 16, 0>(c, fa, fb, acc, aq);
        epi_qk(aq, QSCALE, c.sd);
        c.so[0] = WS_Q + (cg - 7) * 4096 + H * 2048 + tt * 28672 + lane * 16;
    }
    c.so[1] = c.so[0] + 1024;
    STAMP(12);
    groups<H, 16, 35, 0>(c, fa, fb, acc, aq);
    STAMP(13);
    if constexpr (H == 0) asm volatile("s_waitcnt vmcnt(0)" : "+v"(c.fq) :: "memory");
    BARRIER();
}

DI float max16(const f32x16& p) {
    float a = fmaxf(p[0], p[1]);
#pragma unroll
    for (int i = 2; i < 16; ++i) a = fmaxf(a, p[i]);
    auto rr = __builtin_amdgcn_permlane32_swap(__float_as_uint(a), __float_as_uint(a), false, false);
    return fmaxf(__uint_as_float(rr[0]), __uint_as_float(rr[1]));
}
#define EXP2(v) __builtin_amdgcn_exp2f(v)
template <class Mid>
DI void attn_part(char* ws, rsrc_t rws, lptr lds, unsigned wid, int lane, unsigned h, unsigned rb, u64 tag, unsigned fh_off, Mid mid STAMP_ARG) {
    const unsigned hh = lane >> 5, va = lane * 16, vv = (lane & 31) > 28 ? OOB : va;
    const unsigned c0 = (7 * h >> 1) * 512;
    const rsrc_t rk = mk_rsrc(ws + WS_K + c0, 64 * 28672 - c0), rv = mk_rsrc(ws + WS_V + h * (64 * 2048), 64 * 2048);
    const unsigned kb = wid * (16 * 28672), vb = wid * (16 * 2048);
    bf16x8 q[4][2], kv[4][4];
    auto ldq = [&](unsigned t, int s) {
        u32x4 v = __builtin_bit_cast(u32x4, ld_sc1(rws, va, WS_Q + c0 + t * 28672 + s * 1024));
        const bool z = s ? (!(h & 1) && hh) : ((h & 1) && !hh);
        v[2 * s] = z ? 0u : v[2 * s]; v[2 * s + 1] = z ? 0u : v[2 * s + 1]; return __builtin_bit_cast(bf16x8, v); };
    auto ldkv = [&](bf16x8 (&d)[4], unsigned t) { d[0] = ld_sc1(rk, va, kb + t * 28672); d[1] = ld_sc1(rk, va, kb + t * 28672 + 1024); d[2] = ld_sc1(rv, vv, vb + t * 2048); d[3] = ld_sc1(rv, vv, vb + t * 2048 + 1024); };
    u32x4 qraw[4][2];
#pragma unroll
    for (int i = 0; i < 4; ++i) { qraw[i][0] = __builtin_bit_cast(u32x4, ld_sc1(rws, va, WS_Q + c0 + (rb * 4 + i) * 28672)); qraw[i][1] = __builtin_bit_cast(u32x4, ld_sc1(rws, va, WS_Q + c0 + (rb * 4 + i) * 28672 + 1024)); }
    const bf16x8 kz0 = ld_sc1(rk, va, 0), kz1 = ld_sc1(rk, va, 1024);
#pragma unroll
    for (int t = 0; t < 3; ++t) ldkv(kv[t], t);
    SBAR();
    mid();
    SBAR();

    const f32x16 zero = {};
    float mx = -INFINITY;
#pragma unroll
    for (int i = 0; i < 4; ++i) {
        const bool z0 = (h & 1) && !hh, z1 = !(h & 1) && hh;
        qraw[i][0][0] = z0 ? 0u : qraw[i][0][0]; qraw[i][0][1] = z0 ? 0u : qraw[i][0][1]; qraw[i][1][2] = z1 ? 0u : qraw[i][1][2]; qraw[i][1][3] = z1 ? 0u : qraw[i][1][3];
        q[i][0] = __builtin_bit_cast(bf16x8, qraw[i][0]); q[i][1] = __builtin_bit_cast(bf16x8, qraw[i][1]);
        f32x16 s = MFMA(kz0, q[i][0], zero); s = MFMA(kz1, q[i][1], s);
#pragma unroll
        for (int r = 0; r < 16; ++r) mx = fmaxf(mx, s[r]);
    }
    { auto rr = __builtin_amdgcn_permlane32_swap(__float_as_uint(mx), __float_as_uint(mx), false, false); mx = -fmaxf(__uint_as_float(rr[0]), __uint_as_float(rr[1])); }
    f32x16 cm;
#pragma unroll
    for (int r = 0; r < 16; ++r) cm[r] = mx;
    PIN(cm);
    f32x16 o[4], sA, sB; u32x4 p0, p1 = {0, 0, 0, 0};
#pragma unroll
    for (int i = 0; i < 4; ++i) o[i] = zero;
    sA = MFMA(kv[0][0], q[0][0], cm); sA = MFMA(kv[0][1], q[0][1], sA);
    bf16x8 vprev = kv[0][3]; kv[3][3] = kv[0][3];
    u32x2 gv; unsigned trv = 0; int tri = 0; (void)trv; (void)tri;
    float tp0 = 0.f, tp1 = 0.f, tp2 = 0.f, tp3 = 0.f;
#define LDKV(a, b) ldkv(a, b)
#define UNIT(SC, SN, I, KN0, KN1, IN, V0, VP1, IP, LOADS) do { \
    SN = MFMA(KN0, q[IN][0], cm); SBAR(); p1[2] = cvtpk(tp0, tp1); p1[3] = cvtpk(tp2, tp3); PIN(p1); SBAR(); SC[0] = EXP2(SC[0]); SC[1] = EXP2(SC[1]); SC[2] = EXP2(SC[2]); SC[3] = EXP2(SC[3]); PIN(SC); SBAR(); \
    SN = MFMA(KN1, q[IN][1], SN); SBAR(); SC[4] = EXP2(SC[4]); SC[5] = EXP2(SC[5]); p0[0] = cvtpk(SC[0], SC[1]); SC[6] = EXP2(SC[6]); SC[7] = EXP2(SC[7]); p0[1] = cvtpk(SC[2], SC[3]); PIN(SC); PIN(p0); SBAR(); \
    o[IP] = MFMA(VP1, __builtin_bit_cast(bf16x8, p1), o[IP]); SBAR(); p0[2] = cvtpk(SC[4], SC[5]); p0[3] = cvtpk(SC[6], SC[7]); PIN(p0); SBAR(); \
    SC[8] = EXP2(SC[8]); SC[9] = EXP2(SC[9]); SC[10] = EXP2(SC[10]); SC[11] = EXP2(SC[11]); PIN(SC); SBAR(); \
    LOADS; \
    o[I] = MFMA(V0, __builtin_bit_cast(bf16x8, p0), o[I]); SBAR(); tp0 = EXP2(SC[12]); tp1 = EXP2(SC[13]); p1[0] = cvtpk(SC[8], SC[9]); tp2 = EXP2(SC[14]); tp3 = EXP2(SC[15]); p1[1] = cvtpk(SC[10], SC[11]); \
    PIN(tp0); PIN(tp1); PIN(tp2); PIN(tp3); PIN(p1); SBAR(); } while (0)
#define TRA() do {} while (0)
#define TILE(B, BN, BP, TOFF) do { TRA(); \
    UNIT(sA, sB, 0, kv[B][0], kv[B][1], 1, kv[B][2], kv[BP][3], 3, \
         { LDKV(kv[BP], (TOFF)); SBAR(); }); \
    UNIT(sB, sA, 1, kv[B][0], kv[B][1], 2, kv[B][2], kv[B][3], 0, {}); \
    UNIT(sA, sB, 2, kv[B][0], kv[B][1], 3, kv[B][2], kv[B][3], 1, {}); \
    UNIT(sB, sA, 3, kv[BN][0], kv[BN][1], 0, kv[B][2], kv[B][3], 2, {}); } while (0)
    STAMP(6);
#pragma unroll 1
    for (int t = 0; t < 16; t += 4) {
        const unsigned to = t + 3;
        TILE(0, 1, 3, to);
        if (t == 0) { waitv<4>(); if (lane == 0) set_flag(rws, fh_off, tag); }
        gv = __builtin_amdgcn_raw_buffer_load_b64(rws, WS_G + (rb * 128 + wid * 32 + (lane & 31)) * 8, 0, 16);
        TILE(1, 2, 0, t < 12 ? to + 1 : 64);
        TILE(2, 3, 1, t < 12 ? to + 2 : 64); TILE(3, 0, 2, t < 12 ? to + 3 : 64);
    }
#undef TILE
#undef UNIT
    (void)vprev;
    float gtok = __uint_as_float(gv[0]);
    if (!__all(gv[1] == (unsigned)tag)) gtok = load_gate(rws, rb * 128 + wid * 32 + (lane & 31), tag);
    STAMP(7);
    p1[2] = cvtpk(tp0, tp1); p1[3] = cvtpk(tp2, tp3);
    o[3] = MFMA(kv[3][3], __builtin_bit_cast(bf16x8, p1), o[3]);
#pragma unroll
    for (int i = 0; i < 4; ++i) if (i != (int)wid) {
        const unsigned sl = wid > (unsigned)i ? wid - 1 : wid;
#pragma unroll
        for (int g = 0; g < 4; ++g) *(LAS f32x4*)(lds + COMB(i) + (sl * 4 + g) * 1024 + va) = (f32x4){o[i][4 * g], o[i][4 * g + 1], o[i][4 * g + 2], o[i][4 * g + 3]};
    }
    f32x16 r = wid == 0 ? o[0] : (wid == 1 ? o[1] : (wid == 2 ? o[2] : o[3]));
    group_barrier(lds + L_CNT, 4);
#pragma unroll
    for (int j = 0; j < 3; ++j)
#pragma unroll
        for (int g = 0; g < 4; ++g) { const f32x4 v = *(LAS f32x4*)(lds + COMB(wid) + (j * 4 + g) * 1024 + va); r[4 * g] += v[0]; r[4 * g + 1] += v[1]; r[4 * g + 2] += v[2]; r[4 * g + 3] += v[3]; }
    STAMP(8);
    bool ok = true;
#pragma unroll
    for (int i = 0; i < 16; ++i) ok &= fabsf(r[i]) < 1.8e19f;
    ok &= !hh || r[12] > 5.4e-20f;
    if (!__all(ok)) {
        wait_flags<6>(rws, WS_FQ + lane * 8, tag, 21 * 128);
        const bf16x8 q0 = ldq(rb * 4 + wid, 0), q1 = ldq(rb * 4 + wid, 1);
        float m = -INFINITY; r = zero;
#pragma unroll 1
        for (int t = 0; t < 64; ++t) {
            const bf16x8 k0 = ld_sc1(rk, va, t * 28672), k1 = ld_sc1(rk, va, t * 28672 + 1024), v0 = ld_sc1(rv, vv, t * 2048), v1 = ld_sc1(rv, vv, t * 2048 + 1024);
            f32x16 s = MFMA(k0, q0, zero); s = MFMA(k1, q1, s);
            const float mn = fmaxf(m, max16(s)), al = EXP2(m - mn); m = mn;
#pragma unroll
            for (int i = 0; i < 16; ++i) { s[i] = EXP2(s[i] - mn); r[i] *= al; }
            r = MFMA(v0, __builtin_bit_cast(bf16x8, pack8(s, 0)), r); r = MFMA(v1, __builtin_bit_cast(bf16x8, pack8(s, 1)), r);
        }
    }
    auto rr = __builtin_amdgcn_permlane32_swap(__float_as_uint(r[12]), __float_as_uint(r[12]), false, false);
    const float inv = gtok * __builtin_amdgcn_rcpf(__uint_as_float(rr[1]));
#pragma unroll
    for (int i = 0; i < 16; ++i) r[i] = (hh && i >= 12) ? 0.f : r[i] * inv;
    const unsigned tt = rb * 4 + wid, oo = WS_O + (tt * 16 + h) * 2048 + va;
    st_sc1(pack8(r, 0), rws, oo); st_sc1(pack8(r, 1), rws, oo + 1024);
    waitv<0>();
    if (lane == 0) set_flag(rws, WS_FO + ((tt >> 1) * 32 + h * 2 + (tt & 1)) * 8, tag);
}

DI void out_issueA(const OutC& c, int i, u32x4 (&a)[4]) {
#pragma unroll
    for (int t = 0; t < 2; ++t)
#pragma unroll
        for (int s = 0; s < 2; ++s)
            gload16_sc1(a[2 * t + s], c.rws, c.va, i < 14 ? WS_H + ((c.rb * 2 + t) * 112 + c.w * 28 + 2 * i + s) * 1024 : WS_O + (((c.rb * 2 + t) * 16 + c.w * 4 + (i - 14)) * 2 + s) * 1024);
}
template <int N> DI void waitv4(u32x4 (&a)[4]) { asm volatile("s_waitcnt vmcnt(%4)" : "+v"(a[0]), "+v"(a[1]), "+v"(a[2]), "+v"(a[3]) : "n"(N) : "memory"); }
constexpr int out_younger(int i) { int n = 0;
    for (int k = i + 1; k < OB_P; ++k) n += 4;
    for (int j = (i - OB_P + 1 > 0 ? i - OB_P + 1 : 0); j < i; ++j) { if (j + OB_NS < 18) n += 4; if (j + OB_P < 14) n += 4; }
    return n; }
template <int I> DI void out_stage(const OutC& c, lptr lds, const unsigned (&woff)[3][2], u32x4 (&af)[OB_P][4], f32x16 (&acc)[2][2]) {
    u32x4(&a)[4] = af[I % OB_P];
    if constexpr (I < 14) waitv4<out_younger(I)>(a); else waitv4<(17 - I) * 4>(a);
    lptr sl = lds + (RG(I % OB_NS) & 65535);
#pragma unroll
    for (int s = 0; s < 2; ++s) {
        bf16x8 wf[2];
#pragma unroll
        for (int cc = 0; cc < 2; ++cc) wf[cc] = cat(lds_tr(sl + woff[RG(I % OB_NS) >> 16][cc] + s * 2048), lds_tr(sl + woff[RG(I % OB_NS) >> 16][cc] + s * 2048 + 1024));
#pragma unroll
        for (int t = 0; t < 2; ++t)
#pragma unroll
            for (int cc = 0; cc < 2; ++cc) acc[t][cc] = MFMA(wf[cc], __builtin_bit_cast(bf16x8, a[2 * t + s]), acc[t][cc]);
    }
    SBAR();
    if constexpr (I + OB_NS < 18) out_issueW(c, I + OB_NS);
    if constexpr (I + OB_P < 14) out_issueA(c, I + OB_P, a);
}
template <int I0, int I1> DI void out_stages(const OutC& c, lptr lds, const unsigned (&woff)[3][2], u32x4 (&af)[OB_P][4], f32x16 (&acc)[2][2]) {
    if constexpr (I0 < I1) { out_stage<I0>(c, lds, woff, af, acc); out_stages<I0 + 1, I1>(c, lds, woff, af, acc); }
}
DI void out_part(const OutC& c, char* ws, uint16_t* out, lptr lds, int lane, unsigned nb, u64 tag STAMP_ARG) {
    const unsigned r32 = lane & 31, hh = lane >> 5, w = c.w, rb = c.rb;
    u32x4 af[OB_P][4];
    if (w == 0) { wait_flags<1>(c.rws, WS_FH + (rb * 64 + (lane < 56 ? lane : 0)) * 8, tag); if (lane == 0) __hip_atomic_store((LAS unsigned*)(lds + L_CNT + 40), 1u, __ATOMIC_RELAXED, __HIP_MEMORY_SCOPE_WORKGROUP); }
    else lds_wait(lds + L_CNT + 40, 1);
    STAMP(4);
#pragma unroll
    for (int i = 0; i < OB_P; ++i) out_issueA(c, i, af[i]);
    f32x16 ab[2][2] = {};
    const unsigned q = (lane & 15) >> 2;
    unsigned woff[3][2];
#pragma unroll
    for (int cc = 0; cc < 2; ++cc)
#pragma unroll
        for (int u = 0; u < 3; ++u) { woff[u][cc] = u * 65536 + w * 4096 + (4 * hh + q) * 128 + ((cc ^ (q >> 1)) << 6) + ((lane >> 4) & 1) * 32 + (lane & 3) * 8; PIN(woff[u][cc]); }
    out_stages<0, 14>(c, lds, woff, af, ab);
    STAMP(5);
#pragma unroll
    for (int t = 0; t < 2; ++t) { const float gm = 1.f - load_gate(c.rws, rb * 64 + t * 32 + r32, tag);
#pragma unroll
        for (int cc = 0; cc < 2; ++cc) { ab[t][cc] *= gm; PIN(ab[t][cc]); } }
    wait_flags<1>(c.rws, WS_FO + (rb * 32 + w * 8 + (lane & 7)) * 8, tag);
    STAMP(6);
#pragma unroll
    for (int i = 14; i < 18; ++i) out_issueA(c, i, af[i % OB_P]);
    out_stages<14, 18>(c, lds, woff, af, ab);
#pragma unroll
    for (int t = 0; t < 2; ++t)
#pragma unroll
        for (int cc = 0; cc < 2; ++cc)
#pragma unroll
            for (int g = 0; g < 4; ++g)
                *(LAS f32x4*)(lds + RG(2 * t + cc) + w * 4096 + g * 1024 + lane * 16) = (f32x4){ab[t][cc][4 * g], ab[t][cc][4 * g + 1], ab[t][cc][4 * g + 2], ab[t][cc][4 * g + 3]};
    group_barrier(lds + L_CNT + 16, 4);
    uint16_t* orow = out + (size_t)(rb * 64 + (w >> 1) * 32 + r32) * DM + nb * 64 + (w & 1) * 32 + 4 * hh;
#pragma unroll
    for (int g = 0; g < 4; ++g) {
        const unsigned rg = w == 0 ? RG(0) : (w == 1 ? RG(1) : (w == 2 ? RG(2) : RG(3)));
        f32x4 s = *(LAS f32x4*)(lds + rg + g * 1024 + lane * 16);
#pragma unroll
        for (int v = 1; v < 4; ++v) s += *(LAS f32x4*)(lds + rg + v * 4096 + g * 1024 + lane * 16);
        *(u32x2*)(orow + 8 * g) = (u32x2){cvtpk(s[0], s[1]), cvtpk(s[2], s[3])};
    }
}

__global__ __launch_bounds__(512, 2) void fused(const uint16_t* x, const uint16_t* wqkv, const uint16_t* wo, const uint16_t* wg, const uint16_t* wu,
                                                const uint16_t* wd, const uint16_t* gw, char* ws, uint16_t* out) {
    extern __shared__ __attribute__((aligned(16))) char smem[];
    lptr lds = (lptr)smem;
    u64 pc; asm volatile("s_getpc_b64 %0" : "=s"(pc)); pc &= ~(u64)127;
    const int tid = threadIdx.x, lane = tid & 63; const unsigned wid = rfl(tid >> 6);
    const unsigned b = blockIdx.x, xc = b & 7, j = b >> 3;
    STAMP_DECL; STAMP(0);
    u64 tag = *(const __attribute__((address_space(4))) u64*)(ws + WS_E);
    tag += 0x9E3779B97F4A7C15ull; tag = (tag ^ (tag >> 30)) * 0xBF58476D1CE4E5B9ull; tag = (tag ^ (tag >> 27)) * 0x94D049BB133111EBull; tag ^= tag >> 31;
    const rsrc_t rws = mk_rsrc(ws, WS_END);
    OutC oc; oc.rd = mk_rsrc(wd, FF * DM * 2); oc.ro = mk_rsrc(wo, DM * DM * 2); oc.rws = rws; oc.w = wid & 3; oc.rb = xc * 4 + (j & 3);
    oc.l0 = (unsigned)(uintptr_t)lds + oc.w * 4096; oc.va = lane * 16;
    oc.vw = j < 28 ? (lane >> 3) * (DM * 2) + (j >> 2) * 128 + (((lane & 7) ^ (((lane >> 4) & 1) << 2)) << 4) : OOB;
    const unsigned rb = 8 * (xc & 1) + (j & 7), cg = 4 * (j >> 3) + (xc >> 1);
    const unsigned rp = wid & 1, pi = wid >> 1, rt = wid & 3, half = wid >> 2;
    const unsigned h = 2 * xc + (j & 1), ab = j >> 1;
    P1 c; c.lds = lds; c.l0 = (unsigned)(uintptr_t)lds + wid * 2048; c.lane = lane; c.oc = oc; c.rws = rws; c.tag = tag;
    {
        const unsigned xr = 16 * wid + (lane >> 3), qb = cg < 7 ? 14 + cg : cg - 7;
        c.sx.r = mk_rsrc(x, SEQ * DM * 2); c.sx.step = 128;
        c.sx.v0 = (rb * 128 + xr) * (DM * 2) + (((lane & 7) ^ ((xr >> 1) & 7)) << 4); c.sx.v1 = (rb * 128 + xr + 8) * (DM * 2) + (((lane & 7) ^ (((xr + 8) >> 1) & 7)) << 4);
        issue2<0>(c);
        const bool ff = cg < 14; const unsigned pitch = ff ? FF * 2 : 3 * DM * 2;
        c.sf = mk_wsrc(mk_rsrc(ff ? ((wid & 2) ? wu : wg) : wqkv, DM * pitch), pitch, (ff ? 2 * cg + half : 4 * cg - 49 + pi) * 128, 16 * rp, 32, lane);
        issue2<20>(c);
        __builtin_amdgcn_s_barrier();
        issue2<21>(c); issue2<1>(c);
        {
            dma4(c.sf.r, lane < 20 ? (128 + ((j & 7) * 2 + rp) * 20 + lane) * pitch + (ff ? 2 * cg + half : 4 * cg - 49 + pi) * 128 : OOB, c.l0 + it_reg(0, 22));
            if (NTOUCH > 1) dma4(mk_rsrc((const void*)pc, CODE_PF), lane < 2 ? ((j * 8 + wid) * 2 + lane) * 128 : OOB, c.l0 + it_reg(0, 22));
        }
        c.sq = mk_wsrc(mk_rsrc(wqkv, ff ? DM * 3 * DM * 2 : 0), 3 * DM * 2, qb * 128, 16 * wid, 128, lane);
        const unsigned r32 = lane & 31, hh = lane >> 5, q = (lane & 15) >> 2, wl = (8 * hh + q) * 128 + ((lane >> 4) & 1) * 32 + (lane & 3) * 8;
        c.xsw = hh ^ ((r32 >> 1) & 7);
#pragma unroll
        for (int u = 0; u < 3; ++u) {
            c.xf[u] = u * 65536 + (64 * rp + r32) * 128; c.xq[u] = u * 65536 + (32 * rt + r32) * 128;
            c.wf[u] = u * 65536 + half * 8192 + wl + (((pi & 1) ^ (q >> 1)) << 6); c.wq[u] = u * 65536 + wl + ((half ^ (q >> 1)) << 6);
            PIN(c.xf[u]); PIN(c.xq[u]); PIN(c.wf[u]); PIN(c.wq[u]);
        }
    }
    f32x16 acc[4];
    const unsigned fh = cg < 14 ? WS_FH + ((rb * 2 + rp) * 64 + cg * 4 + pi) * 8 : OOB, ho = cg < 14 ? WS_H + ((rb * 4 + 2 * rp) * 112 + (cg * 4 + pi) * 2) * 1024 + lane * 16 : OOB;
    if (wid < 4) {
        const unsigned hb = (28 * h + ((lane & 4) ? 27 : 0)) >> 6, grp = (lane >> 3) & 3;
        c.pollo = WS_FQ + ((grp == 3 ? hb : (grp == 1 ? 14 + hb : 7 + hb)) * 16 + (grp < 2 ? rt * 4 + (lane & 3) : (grp == 2 ? 0 : ab))) * 8;
        c.fqo = (tid == 0 && cg < 14) ? WS_FQ + ((cg < 7 ? 14 + cg : cg - 7) * 16 + rb) * 8 : OOB;
        phase1<0>(c, acc, cg, rb, wid STAMP_PASS);
        STAMP(2);
        if (!__all((c.fq[0] == (unsigned)tag) & (c.fq[1] == (unsigned)(tag >> 32)))) wait_flags<1>(rws, c.pollo, tag);
        STAMP(4);
        attn_part(ws, rws, lds, wid, lane, h, ab, tag, fh, [&]() { if (cg < 14) store_h(acc, ho, rws); } STAMP_PASS);
        STAMP(5);
        if (b == 0 && wid == 0) {
            wait_flags<16>(rws, WS_FO + lane * 8, tag);
            if (lane == 0) set_flag(rws, WS_E, tag);
        }
        STAMP_DUMP();
        __builtin_amdgcn_endpgm();
    }
    {
        phase1<1>(c, acc, cg, rb, wid STAMP_PASS);
        if (cg < 14) { store_h(acc, ho, rws); waitv<0>(); if (lane == 0) set_flag(rws, fh, tag); }
        STAMP(3);
        if (j < 28) out_part(oc, ws, out, lds, lane, j >> 2, tag STAMP_PASS);
        else
        if (j >= 28) {
            const unsigned row = ((j - 28) * 8 + xc) * 64 + (tid - 256) / 4, part = tid & 3;
            const u32x4* xr = (const u32x4*)(x + (size_t)row * DM); const u32x4* gr = (const u32x4*)gw;
            float sum = 0.f;
#pragma unroll
            for (int i = 0; i < 14; ++i) {
                const u32x4 av = xr[i * 4 + part], bv = gr[i * 4 + part];
#pragma unroll
                for (int e = 0; e < 4; ++e) { sum += __uint_as_float(av[e] << 16) * __uint_as_float(bv[e] << 16); sum += __uint_as_float(av[e] & 0xffff0000u) * __uint_as_float(bv[e] & 0xffff0000u); }
            }
            sum += __shfl_xor(sum, 1); sum += __shfl_xor(sum, 2);
            if (part == 0) __builtin_amdgcn_raw_buffer_store_b64((u32x2){__float_as_uint(sigmoidf_(sum)), (unsigned)tag}, rws, WS_G + row * 8, 0, 16);
        }
        STAMP(7);
        STAMP_DUMP();
        __builtin_amdgcn_endpgm();
    }
}

extern "C" void kernel_launch(void* const* d_in, const int* in_sizes, int n_in, void* d_out, int out_size, void* d_ws, size_t ws_size, hipStream_t stream) {
    (void)hipFuncSetAttribute((const void*)fused, hipFuncAttributeMaxDynamicSharedMemorySize, LDS_BYTES);
    fused<<<256, 512, LDS_BYTES, stream>>>((const uint16_t*)d_in[0], (const uint16_t*)d_in[1], (const uint16_t*)d_in[2], (const uint16_t*)d_in[4], (const uint16_t*)d_in[5],
                                           (const uint16_t*)d_in[6], (const uint16_t*)d_in[7], (char*)d_ws, (uint16_t*)d_out);
}
```

```cpp
#include <hip/hip_runtime.h>
#include <hip/hip_bf16.h>
#include <stdint.h>

typedef short bf16x8 __attribute__((ext_vector_type(8)));
typedef short s16x4 __attribute__((ext_vector_type(4)));
typedef float f32x16 __attribute__((ext_vector_type(16)));
typedef float f32x4 __attribute__((ext_vector_type(4)));
typedef float f32x2 __attribute__((ext_vector_type(2)));
typedef unsigned u32x4 __attribute__((ext_vector_type(4)));
typedef unsigned u32x2 __attribute__((ext_vector_type(2)));
typedef __bf16 bf16x2_t __attribute__((ext_vector_type(2)));
typedef unsigned long long u64;
typedef __amdgpu_buffer_rsrc_t rsrc_t;
#define LAS __attribute__((address_space(3)))
#define DI __device__ __forceinline__
typedef LAS char* lptr;

constexpr int SEQ = 2048, DM = 448, DH = 28, FF = 1792;
constexpr float LOG2E = 1.4426950408889634f;
constexpr float QSCALE = 0.18898223650461362f * LOG2E;

constexpr unsigned WS_Q = 0;
constexpr unsigned WS_K = WS_Q + (2u << 20);
constexpr unsigned WS_V = WS_K + (2u << 20);
constexpr unsigned WS_O = WS_V + (2u << 20);
constexpr unsigned WS_H = WS_O + (2u << 20);
constexpr unsigned WS_G = WS_H + 64u * 112 * 1024;
constexpr unsigned WS_FQ = WS_G + 16384;
constexpr unsigned WS_FH = WS_FQ + 8192;
constexpr unsigned WS_FO = WS_FH + 16384;
constexpr unsigned WS_E = WS_FO + 8192;
constexpr unsigned WS_END = WS_E + 256;
constexpr unsigned CODE_PF = 50000;
#define ROT 0
#define PSLEEP 10
#define SIGPOS 0
#define PAUSE 7
#define BWAIT 1
#define NTOUCH 2
constexpr unsigned OOB = 0x7ffffff0u;

constexpr int LDS_BYTES = 160 * 1024;
constexpr unsigned RG(int i) { const int t[6] = {4, 0, 1, 3, 7, 2}; return t[i] * 16384; }
#define COMB(w) (((0x8596u >> (4 * (w))) & 15u) << 14)
constexpr unsigned L_CNT = 6 * 16384 + 12288;

#define MFMA(a, b, c) __builtin_amdgcn_mfma_f32_32x32x16_bf16((a), (b), (c), 0, 0, 0)
#define SBAR() __builtin_amdgcn_sched_barrier(0)
#define PIN(x) asm volatile("" : "+v"(x))
#define BARRIER() asm volatile("s_waitcnt lgkmcnt(0)\n\ts_barrier" ::: "memory")
template <int N> DI void waitv() { asm volatile("s_waitcnt vmcnt(%0)" :: "n"(N) : "memory"); }

#define STAMP_DECL do {} while (0)
#define STAMP(i) do {} while (0)
#define STAMP_DUMP() do {} while (0)
#define STAMP_ARG
#define STAMP_PASS

DI rsrc_t mk_rsrc(const void* p, unsigned bytes) { return __builtin_amdgcn_make_buffer_rsrc((void*)p, (short)0, (int)bytes, 0x00020000); }
DI unsigned rfl(unsigned v) { return (unsigned)__builtin_amdgcn_readfirstlane((int)v); }
DI void dma16(rsrc_t r, unsigned voff, unsigned soff, unsigned ldsaddr) {
    asm volatile("s_mov_b32 m0, %0\n\ts_nop 0\n\tbuffer_load_dwordx4 %1, %2, %3 offen lds" :: "s"(ldsaddr), "v"(voff), "s"(r), "s"(soff) : "memory");
}
DI void dma4(rsrc_t r, unsigned voff, unsigned ldsaddr) {
    asm volatile("s_mov_b32 m0, %0\n\ts_nop 0\n\tbuffer_load_dword %1, %2, 0 offen lds" :: "s"(ldsaddr), "v"(voff), "s"(r) : "memory");
}
DI void gload16_sc1(u32x4& dst, rsrc_t r, unsigned voff, unsigned soff) {
    asm volatile("buffer_load_dwordx4 %0, %1, %2, %3 offen sc1" : "=v"(dst) : "v"(voff), "s"(r), "s"(soff) : "memory");
}
DI bf16x8 ld_sc1(rsrc_t r, unsigned voff, unsigned soff) { return __builtin_bit_cast(bf16x8, __builtin_amdgcn_raw_buffer_load_b128(r, voff, soff, 16)); }
DI void st_sc1(u32x4 v, rsrc_t r, unsigned voff) { __builtin_amdgcn_raw_buffer_store_b128(v, r, voff, 0, 16); }
DI unsigned cvtpk(float lo, float hi) { return __builtin_bit_cast(unsigned, __builtin_convertvector((f32x2){lo, hi}, bf16x2_t)); }
DI bf16x8 lds_b128(lptr p) { return *(const LAS bf16x8*)p; }
DI s16x4 lds_tr(lptr p) { return __builtin_amdgcn_ds_read_tr16_b64_v4i16((LAS s16x4*)p); }
DI bf16x8 cat(s16x4 lo, s16x4 hi) { return __builtin_shufflevector(lo, hi, 0, 1, 2, 3, 4, 5, 6, 7); }
DI u32x4 pack8(const f32x16& a, int s) {
    u32x4 r; r[0] = cvtpk(a[8 * s], a[8 * s + 1]); r[1] = cvtpk(a[8 * s + 2], a[8 * s + 3]); r[2] = cvtpk(a[8 * s + 4], a[8 * s + 5]); r[3] = cvtpk(a[8 * s + 6], a[8 * s + 7]); return r;
}
DI float sigmoidf_(float v) { return __builtin_amdgcn_rcpf(1.f + __builtin_amdgcn_exp2f(-LOG2E * v)); }

DI void set_flag(rsrc_t rws, unsigned off, u64 tag) {
    __builtin_amdgcn_raw_buffer_store_b64((u32x2){(unsigned)tag, (unsigned)(tag >> 32)}, rws, off, 0, 16);
}
template <int NW, int SLEEP = 4> DI void wait_flags(rsrc_t rws, unsigned off, u64 tag, unsigned lim = ~0u) {
#pragma unroll 1
    for (int spin = 0; spin < 40000; ++spin) {
        bool ok = true;
#pragma unroll
        for (int i = 0; i < NW; ++i) { const u32x2 v = __builtin_amdgcn_raw_buffer_load_b64(rws, off + i * 512, 0, 16); ok &= ((v[0] == (unsigned)tag) & (v[1] == (unsigned)(tag >> 32))) | (off + i * 512 - WS_FQ >= lim); }
        if (__all(ok)) break;
        __builtin_amdgcn_s_sleep(SLEEP);
    }
    asm volatile("" ::: "memory");
}
DI float load_gate(rsrc_t rws, unsigned tok, u64 tag) {
    u32x2 v;
#pragma unroll 1
    for (int spin = 0; spin < 40000; ++spin) {
        v = __builtin_amdgcn_raw_buffer_load_b64(rws, WS_G + tok * 8, 0, 16);
        if (__all(v[1] == (unsigned)tag)) break;
        __builtin_amdgcn_s_sleep(2);
    }
    return __uint_as_float(v[0]);
}
DI void signal_last(lptr cnt, rsrc_t rws, unsigned off, u64 tag, int lane) {
    unsigned old = 0;
    if (lane == 0) old = __hip_atomic_fetch_add((LAS unsigned*)cnt, 1u, __ATOMIC_RELAXED, __HIP_MEMORY_SCOPE_WORKGROUP);
    if (rfl(old) == 7) set_flag(rws, lane == 0 ? off : OOB, tag);
}
DI void lds_wait(lptr cnt, unsigned target) {
#pragma unroll 1
    for (int spin = 0; spin < 4000000; ++spin) {
        if (rfl(__hip_atomic_load((LAS unsigned*)cnt, __ATOMIC_RELAXED, __HIP_MEMORY_SCOPE_WORKGROUP)) >= target) break;
        __builtin_amdgcn_s_sleep(4);
    }
    asm volatile("" ::: "memory");
}
DI void group_barrier(lptr words, unsigned w) {
    asm volatile("s_waitcnt lgkmcnt(0)" ::: "memory");
    *(volatile LAS unsigned*)(words + w * 4) = 1u;
#pragma unroll 1
    for (int spin = 0; spin < 4000000; ++spin) {
        const u32x4 v = *(volatile LAS u32x4*)words;
        if (rfl(v[0] & v[1] & v[2] & v[3])) break;
    }
    asm volatile("" ::: "memory");
}

struct WSrc { rsrc_t r; unsigned v0, v1, step; };
DI void issueW(const WSrc& s, int st, unsigned dst) { dma16(s.r, s.v0, st * s.step, dst); dma16(s.r, s.v1, st * s.step, dst + 1024); }
DI WSrc mk_wsrc(rsrc_t r, unsigned pitch, unsigned colb, unsigned row0, unsigned rows_per_stage, int lane) {
    WSrc s; s.r = r; const unsigned sw = ((lane & 7) ^ (((lane >> 4) & 1) << 2)) << 4;
    s.v0 = (row0 + (lane >> 3)) * pitch + colb + sw; s.v1 = s.v0 + 8 * pitch; s.step = rows_per_stage * pitch; return s;
}
#define BPRIO 0
constexpr int OB_NS = 6, OB_P = 4;
struct OutC { rsrc_t rd, ro, rws; unsigned l0, vw, va, w, rb; };
DI void out_issueW1(const OutC& c, int i, int p) {
    const unsigned so = i < 14 ? (c.w * 448 + i * 32) * (DM * 2) : (c.w * 4 + (i - 14)) * DH * (DM * 2);
    dma16(i < 14 ? c.rd : c.ro, c.vw, so + p * 8 * (DM * 2), c.l0 + RG(i % OB_NS) + p * 1024);
}
DI void out_issueW(const OutC& c, int i) {
#pragma unroll
    for (int p = 0; p < 4; ++p) out_issueW1(c, i, p);
}
constexpr unsigned it_reg(int P, int it) { const int x[7] = {0, 3, 7, 8, 1, 2, 5}, q[4] = {6, 9, 4, 0}, f[14] = {1, 2, 4, 5, 3, 6, 9, 4, 0, 3, 7, 6, 9, 8};
    const int x1[7] = {0, 3, 6, 9, 2, 0, 3}, f1[14] = {1, 2, 4, 5, 7, 8, 1, 4, 5, 7, 8, 1, 4, 5};
    return (P ? (it < 10 ? x1[it] : f1[it - 20]) : (it < 10 ? x[it] : (it < 20 ? q[it - 10] : f[it - 20]))) * 16384; }
constexpr int it_first(int P, int it) { const int x[7] = {0, 2, 5, 5, 6, 6, 7};
    return it >= 40 ? 99 : (P ? (it < 10 ? 2 * it : it - 20) : (it < 10 ? x[it] : (it < 20 ? it - 6 : (it < 24 ? it - 20 : it - 16)))); }
constexpr int g_bar(int P, int g) { return (P || g <= 13) ? (((g & 1) && g < 27) ? (g + 1) / 2 : -1) : ((g & 1) || g > 32 ? -1 : (g + 2) / 2); }
constexpr int g_item(int P, int g, int u) {
    const int t[23] = {22, 10, 2, 3, 11, 4, 5, 12, 6, 13, 24, 25, 26, 27, 28, -1, 29, -1, 30, 31, 32, -1, 33};
    const int t1[23] = {22, 2, 24, 25, 3, 26, 4, 27, 5, 28, 29, 6, 30, 31, -1, 32, -1, 33, -1, -1, -1, -1, -1};
    return u ? (g == 0 ? 23 : -1) : (g < 23 ? (P ? t1[g] : t[g]) : -1); }
constexpr int g_dd(int P, int H, int g) { return (H == 1 && !P) ? (g == 23 ? 0 : (g == 24 ? 1 : (g == 26 ? 2 : (g == 27 ? 3 : (g == 28 ? 4 : (g == 30 ? 5 : -1)))))) : -1; }
constexpr int g_st(int P, int g) { return (!P && g >= 16 && g < 19) ? g - 16 : -1; }
constexpr int ST_PUB = 14;
constexpr int vm_count(int P, int H, int gs) {
    int tag[96] = {}, cnt[96] = {}, m = 0; const int n = gs < 0 ? 0 : g_bar(P, gs);
    { const int t[4] = {0, 20, 21, 1}; for (int i = 0; i < 4; ++i) { tag[m] = t[i]; cnt[m++] = 2; } tag[m] = 97; cnt[m++] = NTOUCH; }
    for (int g = 0; g < gs; ++g) {
        for (int u = 0; u < 2; ++u) if (g_item(P, g, u) >= 0) { tag[m] = g_item(P, g, u); cnt[m++] = 2; }
        if (g_dd(P, H, g) >= 0) { tag[m] = 40; cnt[m++] = 4; }
        if (g_st(P, g) >= 0) { tag[m] = 98; cnt[m++] = 1; }
    }
    int last = -1;
    for (int i = 0; i < m; ++i) if (it_first(P, tag[i]) <= n || (tag[i] == 98 && n == ST_PUB)) last = i;
    int r = 0; for (int i = last + 1; i < m; ++i) r += cnt[i];
    return r;
}
struct P1 { lptr lds; unsigned l0, xsw; int lane; WSrc sx, sq, sf; OutC oc; unsigned xf[3], wf[3], xq[3], wq[3]; rsrc_t rws; u64 tag; u32x4 sd[3]; unsigned so[3], fqo, pollo; u32x2 fq; };
template <int P, int IT> DI void issue1(const P1& c, int p) {
    const WSrc& s = IT < 10 ? c.sx : (IT < 20 ? c.sq : c.sf);
    dma16(s.r, p ? s.v1 : s.v0, (IT < 10 ? IT : (IT < 20 ? IT - 10 : IT - 20)) * s.step, c.l0 + it_reg(P, IT) + p * 1024);
}
template <int IT> DI void issue2(const P1& c) { issue1<0, IT>(c, 0); issue1<0, IT>(c, 1); }
struct Frags { bf16x8 x[4], w[4]; };
DI lptr at(const P1& c, const unsigned (&b)[3], unsigned off) { return c.lds + b[off >> 16] + (off & 65535); }
template <int P, int G> DI void rd_part(Frags& f, const P1& c, int i) {
    if constexpr (P || G < 8 || G >= 15) {
        constexpr int K = (P || G < 8) ? G : G - 7;
        f.x[i] = lds_b128(at(c, c.xf, it_reg(P, K >> 2) + i * 4096) + (((2 * (K & 3)) ^ c.xsw) << 4));
        lptr wp = at(c, c.wf, it_reg(P, 20 + (K >> 1)) + (K & 1) * 2048 + i * 4096);
        f.w[i] = cat(lds_tr(wp), lds_tr(wp + 512));
    } else {
#pragma unroll
        for (int u = 2 * i; u < 2 * i + 2; ++u) {
            f.x[u] = lds_b128(at(c, c.xq, it_reg(0, G - 8)) + (((2 * u) ^ c.xsw) << 4));
            lptr wp = at(c, c.wq, it_reg(0, 10 + ((G - 8) >> 1)) + ((G & 1) * 4 + u) * 2048);
            f.w[u] = cat(lds_tr(wp), lds_tr(wp + 512));
        }
    }
}
template <int H, int G, int MODE> DI void group(P1& c, Frags& fc, Frags& fn, f32x16 (&acc)[4], f32x16& aq) {
    constexpr int P = MODE == 2; constexpr bool NEXT = G + 1 < (P ? 28 : 35), QG = !P && G >= 8 && G < 15;
    constexpr int I0 = g_item(P, G, 0), I1 = g_item(P, G, 1), DD = g_dd(P, H, G), ST = g_st(P, G);
    if constexpr (g_bar(P, G) >= 0) {
        waitv<vm_count(P, H, G)>(); BARRIER();
        if constexpr (H == 0 && g_bar(P, G) == ST_PUB) set_flag(c.rws, c.fqo, c.tag);
        if constexpr (H == 0 && G == 32) asm volatile("buffer_load_dwordx2 %0, %1, %2, 0 offen sc1" : "=v"(c.fq) : "v"(c.pollo), "s"(c.rws) : "memory");
    }
    if constexpr (H == 1 && G == 31) { if (c.lane < 32) *(LAS unsigned*)(c.lds + L_CNT + c.lane * 4) = 0; }
    SBAR();
#pragma unroll
    for (int i = 0; i < 4; ++i) {
        if constexpr (!QG) acc[i] = MFMA(fc.w[i & 1], fc.x[i >> 1], acc[i]);
        else if constexpr (MODE == 1) aq = MFMA(fc.x[i], fc.w[i], aq);
        else if constexpr (MODE == 0) aq = MFMA(fc.w[i], fc.x[i], aq);
        if constexpr (NEXT) { if (i < 2) rd_part<P, NEXT ? G + 1 : G>(fn, c, i); }
        SBAR();
        if constexpr (I1 >= 0) { if (i < 2) issue1<P, I0 >= 0 ? I0 : 0>(c, i); else issue1<P, I1 >= 0 ? I1 : 0>(c, i - 2); SBAR(); }
        else if constexpr (I0 >= 0) { if ((i >> 1) == H) { issue1<P, I0 >= 0 ? I0 : 0>(c, i & 1); SBAR(); } }
        if constexpr (DD >= 0) { out_issueW1(c.oc, DD >= 0 ? DD : 0, i); SBAR(); }
        if constexpr (ST >= 0) { if (i == 2 * H + 1) { st_sc1(c.sd[ST >= 0 ? ST : 0], c.rws, c.so[ST >= 0 ? ST : 0]); SBAR(); } }
    }
}
template <int H, int G0, int G1, int MODE> DI void groups(P1& c, Frags& fa, Frags& fb, f32x16 (&acc)[4], f32x16& aq) {
    if constexpr (G0 < G1) {
        if constexpr (G0 & 1) group<H, G0, MODE>(c, fb, fa, acc, aq); else group<H, G0, MODE>(c, fa, fb, acc, aq);
        groups<H, G0 + 1, G1, MODE>(c, fa, fb, acc, aq);
    }
}
DI void epi_qk(const f32x16& a, float sc, u32x4* sd) {
#pragma unroll
    for (int p = 0; p < 2; ++p) {
        unsigned a0 = cvtpk(a[8 * p] * sc, a[8 * p + 1] * sc), a1 = cvtpk(a[8 * p + 2] * sc, a[8 * p + 3] * sc), b0 = cvtpk(a[8 * p + 4] * sc, a[8 * p + 5] * sc), b1 = cvtpk(a[8 * p + 6] * sc, a[8 * p + 7] * sc);
        auto s0 = __builtin_amdgcn_permlane32_swap(a0, b0, false, false); auto s1 = __builtin_amdgcn_permlane32_swap(a1, b1, false, false);
        sd[p] = (u32x4){s0[0], s1[0], s0[1], s1[1]};
    }
}
DI void epi_h(const f32x16& g, const f32x16& u, u32x4* d) {
    f32x16 hv;
#pragma unroll
    for (int i = 0; i < 16; ++i) hv[i] = u[i] * sigmoidf_(g[i]);
    d[0] = pack8(hv, 0); d[1] = pack8(hv, 1);
}
DI void store_h(const u32x4 (&d)[4], unsigned o, rsrc_t rws) {
#pragma unroll
    for (int i = 0; i < 4; ++i) st_sc1(d[i], rws, o + (i >> 1) * 112 * 1024 + (i & 1) * 1024);
}
template <int H> DI void phase1(P1& c, f32x16 (&acc)[4], unsigned cg, unsigned rb, unsigned wid STAMP_ARG) {
    Frags fa, fb; f32x16 aq = {};
    const int lane = c.lane; const rsrc_t rws = c.rws;
#pragma unroll
    for (int i = 0; i < 4; ++i) acc[i] = (f32x16){};
    STAMP(9);
    waitv<vm_count(0, H, -1)>(); BARRIER();
    STAMP(10);
    const unsigned rp = wid & 1, pi = wid >> 1, tt = rb * 4 + (wid & 3);
    if (cg >= 14) {
        asm volatile("" ::: "memory");
        rd_part<0, 0>(fa, c, 0); rd_part<0, 0>(fa, c, 1);
        groups<H, 0, 28, 2>(c, fa, fb, acc, aq);
        STAMP(13);
#pragma unroll
        for (int i = 0; i < 4; ++i) { u32x4 sd[2]; const unsigned kb = 4 * cg - 56 + H * 2 + (i & 1), o = WS_K + kb * 4096 + (pi & 1) * 2048 + (rb * 4 + 2 * rp + (i >> 1)) * 28672 + lane * 16;
            epi_qk(acc[i], 1.f, sd);
            if (kb < 7) { st_sc1(sd[0], rws, o); st_sc1(sd[1], rws, o + 1024); }
            else __builtin_amdgcn_raw_buffer_store_b64((u32x2){__float_as_uint(sigmoidf_(acc[i][0] + acc[i][5])), (unsigned)c.tag}, rws,
                                                       ((pi & 1) == 0 && lane < 32) ? WS_G + ((rb * 4 + 2 * rp + (i >> 1)) * 32 + lane) * 8 : OOB, 0, 16); }
        if (H == 1) { if (lane < 32) *(LAS unsigned*)(c.lds + L_CNT + lane * 4) = 0; }
        waitv<0>();
        BARRIER();
        if (H == 0) { c.fq = (u32x2){0, 0}; if (wid == 0 && lane < 4 && 4 * cg - 56 + lane < 7) set_flag(rws, WS_FQ + ((4 * cg - 49 + lane) * 16 + rb) * 8, c.tag); }
        else {
#pragma unroll
            for (int i = 0; i < OB_NS; ++i) out_issueW(c.oc, i);
        }
        return;
    }
    asm volatile("" ::: "memory");
    rd_part<0, 0>(fa, c, 0); rd_part<0, 0>(fa, c, 1);
    groups<H, 0, 8, 0>(c, fa, fb, acc, aq);
    STAMP(11);
    c.sd[2] = (u32x4){0x3f803f80u, 0x3f803f80u, 0x3f803f80u, 0x3f803f80u}; c.so[2] = OOB;
    if (cg < 7) {
        groups<H, 8, 16, 1>(c, fa, fb, acc, aq);
        const unsigned n = cg * 64 + H * 32 + (lane & 31), hd = n / 28, ph = (cg * 64 + 27) / 28 + (lane >> 2);
        c.sd[0] = pack8(aq, 0); c.sd[1] = pack8(aq, 1);
        c.so[0] = WS_V + (hd * 64 + tt) * 2048 + (n - 28 * hd + (lane & 32)) * 16;
        if (H == 0) c.so[2] = ph * 28 < (cg + 1) * 64 ? WS_V + (ph * 64 + tt) * 2048 + (lane & 1) * 1024 + (28 + 16 * (lane & 2)) * 16 : OOB;
    } else {
        groups<H, 8, 16, 0>(c, fa, fb, acc, aq);
        epi_qk(aq, QSCALE, c.sd);
        c.so[0] = WS_Q + (cg - 7) * 4096 + H * 2048 + tt * 28672 + lane * 16;
    }
    c.so[1] = c.so[0] + 1024;
    STAMP(12);
    groups<H, 16, 35, 0>(c, fa, fb, acc, aq);
    STAMP(13);
    if constexpr (H == 0) asm volatile("s_waitcnt vmcnt(0)" : "+v"(c.fq) :: "memory");
    BARRIER();
}

DI float max16(const f32x16& p) {
    float a = fmaxf(p[0], p[1]);
#pragma unroll
    for (int i = 2; i < 16; ++i) a = fmaxf(a, p[i]);
    auto rr = __builtin_amdgcn_permlane32_swap(__float_as_uint(a), __float_as_uint(a), false, false);
    return fmaxf(__uint_as_float(rr[0]), __uint_as_float(rr[1]));
}
#define EXP2(v) __builtin_amdgcn_exp2f(v)
DI void attn_part(char* ws, rsrc_t rws, lptr lds, unsigned wid, int lane, unsigned h, unsigned rb, u64 tag, unsigned fh_off, unsigned ho, const f32x16 (&acc)[4], bool ready, unsigned pollo STAMP_ARG) {
    const unsigned hh = lane >> 5, va = lane * 16, vv = (lane & 31) > 28 ? OOB : va;
    const unsigned c0 = (7 * h >> 1) * 512;
    const rsrc_t rk = mk_rsrc(ws + WS_K + c0, 64 * 28672 - c0), rv = mk_rsrc(ws + WS_V + h * (64 * 2048), 64 * 2048);
    const unsigned kb = wid * (16 * 28672), vb = wid * (16 * 2048);
    bf16x8 q[4][2], kv[4][4];
    auto ldq = [&](unsigned t, int s) {
        u32x4 v = __builtin_bit_cast(u32x4, ld_sc1(rws, va, WS_Q + c0 + t * 28672 + s * 1024));
        const bool z = s ? (!(h & 1) && hh) : ((h & 1) && !hh);
        v[2 * s] = z ? 0u : v[2 * s]; v[2 * s + 1] = z ? 0u : v[2 * s + 1]; return __builtin_bit_cast(bf16x8, v); };
    auto ldkv = [&](bf16x8 (&d)[4], unsigned t0) { const unsigned t = (t0 + ROT * rb) & 15;
        d[0] = ld_sc1(rk, va, kb + t * 28672); d[1] = ld_sc1(rk, va, kb + t * 28672 + 1024); d[2] = ld_sc1(rv, vv, vb + t * 2048); d[3] = ld_sc1(rv, vv, vb + t * 2048 + 1024); };
    u32x4 qraw[4][2]; bf16x8 kz0, kz1;
    auto first_loads = [&]() {
#pragma unroll
        for (int i = 0; i < 4; ++i) { const unsigned qo = WS_Q + c0 + (rb * 4 + ((wid + 1 + i) & 3)) * 28672;
            qraw[i][0] = __builtin_bit_cast(u32x4, ld_sc1(rws, va, qo)); qraw[i][1] = __builtin_bit_cast(u32x4, ld_sc1(rws, va, qo + 1024)); }
        kz0 = ld_sc1(rk, va, 0); kz1 = ld_sc1(rk, va, 1024);
#pragma unroll
        for (int t = 0; t < 4; ++t) ldkv(kv[t], t);
    };
    if (ready) first_loads();
    SBAR();
    u32x4 hd[4]; epi_h(acc[0], acc[1], hd); epi_h(acc[2], acc[3], hd + 2);
    PIN(hd[0]); PIN(hd[1]); PIN(hd[2]); PIN(hd[3]);
    if (!ready) { wait_flags<1>(rws, pollo, tag); first_loads(); }
    asm volatile("" ::: "memory");
    *(volatile LAS unsigned*)(lds + L_CNT + 32 + wid * 4) = 1u;
    store_h(hd, ho, rws);
    SBAR();
    const f32x16 zero = {};
    float mx = -INFINITY;
#pragma unroll
    for (int i = 0; i < 4; ++i) {
        const bool z0 = (h & 1) && !hh, z1 = !(h & 1) && hh;
        qraw[i][0][0] = z0 ? 0u : qraw[i][0][0]; qraw[i][0][1] = z0 ? 0u : qraw[i][0][1]; qraw[i][1][2] = z1 ? 0u : qraw[i][1][2]; qraw[i][1][3] = z1 ? 0u : qraw[i][1][3];
        q[i][0] = __builtin_bit_cast(bf16x8, qraw[i][0]); q[i][1] = __builtin_bit_cast(bf16x8, qraw[i][1]);
        f32x16 s = MFMA(kz0, q[i][0], zero); s = MFMA(kz1, q[i][1], s);
#pragma unroll
        for (int r = 0; r < 16; ++r) mx = fmaxf(mx, s[r]);
    }
    { auto rr = __builtin_amdgcn_permlane32_swap(__float_as_uint(mx), __float_as_uint(mx), false, false); mx = -fmaxf(__uint_as_float(rr[0]), __uint_as_float(rr[1])); }
    f32x16 cm;
#pragma unroll
    for (int r = 0; r < 16; ++r) cm[r] = mx;
    PIN(cm);
    f32x16 o[4], sA, sB; u32x4 p0, p1 = {0, 0, 0, 0};
#pragma unroll
    for (int i = 0; i < 4; ++i) o[i] = zero;
    sA = MFMA(kv[0][0], q[0][0], cm); sA = MFMA(kv[0][1], q[0][1], sA);
    u32x2 gv; unsigned trv = 0; int tri = 0; (void)trv; (void)tri;
    float tp0 = 0.f, tp1 = 0.f, tp2 = 0.f, tp3 = 0.f;
    auto wr_o = [&](int i) { const unsigned own = (wid + 1 + i) & 3; lptr d = lds + COMB(own) + i * 4096 + va;
#pragma unroll
        for (int g = 0; g < 4; ++g) *(LAS f32x4*)(d + g * 1024) = (f32x4){o[i][4 * g], o[i][4 * g + 1], o[i][4 * g + 2], o[i][4 * g + 3]}; };
#define UNIT(SC, SN, I, KN0, KN1, IN, V0, VP1, IP, LOADS) do { \
    SN = MFMA(KN0, q[IN][0], cm); SBAR(); p1[2] = cvtpk(tp0, tp1); p1[3] = cvtpk(tp2, tp3); PIN(p1); SBAR(); SC[0] = EXP2(SC[0]); SC[1] = EXP2(SC[1]); SC[2] = EXP2(SC[2]); SC[3] = EXP2(SC[3]); PIN(SC); SBAR(); \
    SN = MFMA(KN1, q[IN][1], SN); SBAR(); SC[4] = EXP2(SC[4]); SC[5] = EXP2(SC[5]); p0[0] = cvtpk(SC[0], SC[1]); SC[6] = EXP2(SC[6]); SC[7] = EXP2(SC[7]); p0[1] = cvtpk(SC[2], SC[3]); PIN(SC); PIN(p0); SBAR(); \
    o[IP] = MFMA(VP1, __builtin_bit_cast(bf16x8, p1), o[IP]); SBAR(); p0[2] = cvtpk(SC[4], SC[5]); p0[3] = cvtpk(SC[6], SC[7]); PIN(p0); SBAR(); \
    SC[8] = EXP2(SC[8]); SC[9] = EXP2(SC[9]); SC[10] = EXP2(SC[10]); SC[11] = EXP2(SC[11]); PIN(SC); SBAR(); \
    LOADS; \
    o[I] = MFMA(V0, __builtin_bit_cast(bf16x8, p0), o[I]); SBAR(); tp0 = EXP2(SC[12]); tp1 = EXP2(SC[13]); p1[0] = cvtpk(SC[8], SC[9]); tp2 = EXP2(SC[14]); tp3 = EXP2(SC[15]); p1[1] = cvtpk(SC[10], SC[11]); \
    PIN(tp0); PIN(tp1); PIN(tp2); PIN(tp3); PIN(p1); SBAR(); } while (0)
#define TRA() do {} while (0)
#define TILE(T) do { TRA(); \
    UNIT(sA, sB, 0, kv[(T) & 3][0], kv[(T) & 3][1], 1, kv[(T) & 3][2], kv[(T) ? ((T) + 3) & 3 : 0][3], 3, { if ((T) > 0 && (T) + 3 < 16) { ldkv(kv[((T) + 3) & 3], (T) + 3); SBAR(); } }); \
    UNIT(sB, sA, 1, kv[(T) & 3][0], kv[(T) & 3][1], 2, kv[(T) & 3][2], kv[(T) & 3][3], 0, {}); \
    UNIT(sA, sB, 2, kv[(T) & 3][0], kv[(T) & 3][1], 3, kv[(T) & 3][2], kv[(T) & 3][3], 1, { if ((T) == 15) { wr_o(0); SBAR(); } }); \
    UNIT(sB, sA, 3, kv[((T) + 1) & 3][0], kv[((T) + 1) & 3][1], 0, kv[(T) & 3][2], kv[(T) & 3][3], 2, { if ((T) == 15) { wr_o(1); SBAR(); } }); } while (0)
    STAMP(6);
    TILE(0); TILE(1);
    waitv<4>(); signal_last(lds + L_CNT + 64, rws, fh_off, tag, lane); SBAR();
    TILE(2); TILE(3); TILE(4); TILE(5); TILE(6); TILE(7); TILE(8); TILE(9); TILE(10); TILE(11); TILE(12);
    gv = __builtin_amdgcn_raw_buffer_load_b64(rws, WS_G + (rb * 128 + wid * 32 + (lane & 31)) * 8, 0, 16); SBAR();
    TILE(13); TILE(14);
    if (SIGPOS == 0) *(volatile LAS unsigned*)(lds + L_CNT + 48 + wid * 4) = 1u;
    __builtin_amdgcn_s_setprio(2); SBAR();
    TILE(15);
    if (SIGPOS == 1) *(volatile LAS unsigned*)(lds + L_CNT + 48 + wid * 4) = 1u;
#undef TILE
#undef UNIT
    STAMP(7);
    p1[2] = cvtpk(tp0, tp1); p1[3] = cvtpk(tp2, tp3);
    o[3] = MFMA(kv[3][3], __builtin_bit_cast(bf16x8, p1), o[3]);
    wr_o(2);
    PIN(o[3]);
    STAMP(1);
    group_barrier(lds + L_CNT, wid);
    STAMP(14);
    f32x16 r = o[3];
    float gtok = __uint_as_float(gv[0]);
    if (!__all(gv[1] == (unsigned)tag)) { STAMP(3); gtok = load_gate(rws, rb * 128 + wid * 32 + (lane & 31), tag); }
    STAMP(15);
#pragma unroll
    for (int j = 0; j < 3; ++j)
#pragma unroll
        for (int g = 0; g < 4; ++g) { const f32x4 v = *(LAS f32x4*)(lds + COMB(wid) + (j * 4 + g) * 1024 + va); r[4 * g] += v[0]; r[4 * g + 1] += v[1]; r[4 * g + 2] += v[2]; r[4 * g + 3] += v[3]; }
    STAMP(8);
    bool ok = true;
#pragma unroll
    for (int i = 0; i < 16; ++i) ok &= fabsf(r[i]) < 1.8e19f;
    ok &= !hh || r[12] > 5.4e-20f;
    if (!__all(ok)) {
        wait_flags<6>(rws, WS_FQ + lane * 8, tag, 21 * 128);
        const bf16x8 q0 = ldq(rb * 4 + wid, 0), q1 = ldq(rb * 4 + wid, 1);
        float m = -INFINITY; r = zero;
#pragma unroll 1
        for (int t = 0; t < 64; ++t) {
            const bf16x8 k0 = ld_sc1(rk, va, t * 28672), k1 = ld_sc1(rk, va, t * 28672 + 1024), v0 = ld_sc1(rv, vv, t * 2048), v1 = ld_sc1(rv, vv, t * 2048 + 1024);
            f32x16 s = MFMA(k0, q0, zero); s = MFMA(k1, q1, s);
            const float mn = fmaxf(m, max16(s)), al = EXP2(m - mn); m = mn;
#pragma unroll
            for (int i = 0; i < 16; ++i) { s[i] = EXP2(s[i] - mn); r[i] *= al; }
            r = MFMA(v0, __builtin_bit_cast(bf16x8, pack8(s, 0)), r); r = MFMA(v1, __builtin_bit_cast(bf16x8, pack8(s, 1)), r);
        }
    }
    auto rr = __builtin_amdgcn_permlane32_swap(__float_as_uint(r[12]), __float_as_uint(r[12]), false, false);
    const float inv = gtok * __builtin_amdgcn_rcpf(__uint_as_float(rr[1]));
#pragma unroll
    for (int i = 0; i < 16; ++i) r[i] = (hh && i >= 12) ? 0.f : r[i] * inv;
    const unsigned tt = rb * 4 + wid, oo = WS_O + (tt * 16 + h) * 2048 + va;
    st_sc1(pack8(r, 0), rws, oo); st_sc1(pack8(r, 1), rws, oo + 1024);
    if (SIGPOS == 2) *(volatile LAS unsigned*)(lds + L_CNT + 48 + wid * 4) = 1u;
    waitv<0>();
    if (lane == 0) set_flag(rws, WS_FO + ((tt >> 1) * 32 + h * 2 + (tt & 1)) * 8, tag);
}

DI void out_issueA(const OutC& c, int i, u32x4 (&a)[4]) {
#pragma unroll
    for (int t = 0; t < 2; ++t)
#pragma unroll
        for (int s = 0; s < 2; ++s)
            gload16_sc1(a[2 * t + s], c.rws, c.va, i < 14 ? WS_H + ((c.rb * 2 + (t ^ (c.w >> 1))) * 112 + c.w * 28 + 2 * i + s) * 1024 : WS_O + (((c.rb * 2 + (t ^ (c.w >> 1))) * 16 + c.w * 4 + (i - 14)) * 2 + s) * 1024);
}
template <int N> DI void waitv4(u32x4 (&a)[4]) { asm volatile("s_waitcnt vmcnt(%4)" : "+v"(a[0]), "+v"(a[1]), "+v"(a[2]), "+v"(a[3]) : "n"(N) : "memory"); }
constexpr int out_younger(int i) { int n = 0;
    for (int k = i + 1; k < OB_P; ++k) n += 4;
    for (int j = (i - OB_P + 1 > 0 ? i - OB_P + 1 : 0); j < i; ++j) { if (j + OB_NS < 18) n += 4; if (j + OB_P < 14) n += 4; }
    return n; }
template <int I> DI void out_stage(const OutC& c, lptr lds, const unsigned (&woff)[3][2], u32x4 (&af)[OB_P][4], f32x16 (&acc)[2][2]) {
    u32x4(&a)[4] = af[I % OB_P];
    waitv4<out_younger(I)>(a);
    lptr sl = lds + (RG(I % OB_NS) & 65535);
#pragma unroll
    for (int s = 0; s < 2; ++s) {
        bf16x8 wf[2];
#pragma unroll
        for (int cc = 0; cc < 2; ++cc) wf[cc] = cat(lds_tr(sl + woff[RG(I % OB_NS) >> 16][cc] + s * 2048), lds_tr(sl + woff[RG(I % OB_NS) >> 16][cc] + s * 2048 + 1024));
#pragma unroll
        for (int t = 0; t < 2; ++t)
#pragma unroll
            for (int cc = 0; cc < 2; ++cc) acc[t][cc] = MFMA(wf[cc], __builtin_bit_cast(bf16x8, a[2 * t + s]), acc[t][cc]);
    }
    SBAR();
    if constexpr (I + OB_NS < 18) out_issueW(c, I + OB_NS);
    if constexpr (I + OB_P < 14) out_issueA(c, I + OB_P, a);
}
template <int I0, int I1> DI void out_stages(const OutC& c, lptr lds, const unsigned (&woff)[3][2], u32x4 (&af)[OB_P][4], f32x16 (&acc)[2][2]) {
    if constexpr (I0 < I1) { out_stage<I0>(c, lds, woff, af, acc); out_stages<I0 + 1, I1>(c, lds, woff, af, acc); }
}
DI void out_part(const OutC& c, char* ws, uint16_t* out, lptr lds, int lane, unsigned nb, u64 tag STAMP_ARG) {
    const unsigned r32 = lane & 31, hh = lane >> 5, w = c.w, rb = c.rb;
    u32x4 af[OB_P][4];
    const u32x2 gv0 = __builtin_amdgcn_raw_buffer_load_b64(c.rws, WS_G + (rb * 64 + (w >> 1) * 32 + r32) * 8, 0, 16), gv1 = __builtin_amdgcn_raw_buffer_load_b64(c.rws, WS_G + (rb * 64 + ((w >> 1) ^ 1) * 32 + r32) * 8, 0, 16);
    if (w == 0) {
        lds_wait(lds + L_CNT + 64, 8);
        __builtin_amdgcn_s_sleep(PSLEEP);
        wait_flags<1, 10>(c.rws, WS_FH + ((rb >> 1) * 16 + ((lane & 15) < 14 ? lane & 15 : 0)) * 8, tag);
        *(volatile LAS unsigned*)(lds + L_CNT + 68) = 1u;
    } else lds_wait(lds + L_CNT + 68, 1);
    STAMP(4);
#pragma unroll
    for (int i = 0; i < OB_P; ++i) out_issueA(c, i, af[i]);
    f32x16 ab[2][2] = {};
    const unsigned q = (lane & 15) >> 2;
    unsigned woff[3][2];
#pragma unroll
    for (int cc = 0; cc < 2; ++cc)
#pragma unroll
        for (int u = 0; u < 3; ++u) { woff[u][cc] = u * 65536 + w * 4096 + (4 * hh + q) * 128 + ((cc ^ (w & 1) ^ (q >> 1)) << 6) + ((lane >> 4) & 1) * 32 + (lane & 3) * 8; PIN(woff[u][cc]); }
    __builtin_amdgcn_s_setprio(BPRIO);
    out_stages<0, PAUSE>(c, lds, woff, af, ab);
    if (PAUSE < 14) {
#pragma unroll 1
        for (int spin = 0; spin < 100000; ++spin) { if (rfl(*(volatile LAS unsigned*)(lds + L_CNT + 48 + w * 4))) break; __builtin_amdgcn_s_sleep(8); }
        asm volatile("" ::: "memory");
    }
    out_stages<PAUSE, 14>(c, lds, woff, af, ab);
    __builtin_amdgcn_s_setprio(1);
    STAMP(5);
    {
        float g0 = __uint_as_float(gv0[0]), g1 = __uint_as_float(gv1[0]);
        if (!__all((gv0[1] == (unsigned)tag) & (gv1[1] == (unsigned)tag))) { g0 = load_gate(c.rws, rb * 64 + (w >> 1) * 32 + r32, tag); g1 = load_gate(c.rws, rb * 64 + ((w >> 1) ^ 1) * 32 + r32, tag); }
#pragma unroll
        for (int cc = 0; cc < 2; ++cc) { ab[0][cc] *= 1.f - g0; ab[1][cc] *= 1.f - g1; PIN(ab[0][cc]); PIN(ab[1][cc]); }
    }
    bf16x8 wo_f[4][2][2];
    waitv<0>();
#pragma unroll
    for (int i = 0; i < 4; ++i)
#pragma unroll
        for (int s = 0; s < 2; ++s)
#pragma unroll
            for (int cc = 0; cc < 2; ++cc) { lptr p = lds + (RG((14 + i) % OB_NS) & 65535) + woff[RG((14 + i) % OB_NS) >> 16][cc] + s * 2048; wo_f[i][s][cc] = cat(lds_tr(p), lds_tr(p + 1024)); PIN(wo_f[i][s][cc]); }
    wait_flags<1>(c.rws, WS_FO + (rb * 32 + w * 8 + (lane & 7)) * 8, tag);
    STAMP(6);
#pragma unroll
    for (int i = 0; i < 4; ++i) out_issueA(c, 14 + i, af[i]);
#define OSTAGE(i) do { waitv4<(3 - (i)) * 4>(af[i]); \
    _Pragma("unroll") for (int s = 0; s < 2; ++s) _Pragma("unroll") for (int l = 3; l >= 0; --l) ab[l >> 1][l & 1] = MFMA(wo_f[i][s][l & 1], __builtin_bit_cast(bf16x8, af[i][2 * (l >> 1) + s]), ab[l >> 1][l & 1]); SBAR(); } while (0)
    OSTAGE(0); STAMP(8); OSTAGE(1); OSTAGE(2); OSTAGE(3);
#undef OSTAGE
    STAMP(14);
#pragma unroll
    for (int l = 3; l >= 1; --l)
#pragma unroll
        for (int g = 0; g < 4; ++g)
            *(LAS f32x4*)(lds + RG(l) + w * 4096 + g * 1024 + lane * 16) = (f32x4){ab[l >> 1][l & 1][4 * g], ab[l >> 1][l & 1][4 * g + 1], ab[l >> 1][l & 1][4 * g + 2], ab[l >> 1][l & 1][4 * g + 3]};
    PIN(ab[0][0]);
    STAMP(15);
    group_barrier(lds + L_CNT + 16, w);
    STAMP(1);
    f32x16 r = ab[0][0];
#pragma unroll
    for (int l = 1; l < 4; ++l)
#pragma unroll
        for (int g = 0; g < 4; ++g) { const f32x4 v = *(LAS f32x4*)(lds + RG(l) + (w ^ l) * 4096 + g * 1024 + lane * 16); r[4 * g] += v[0]; r[4 * g + 1] += v[1]; r[4 * g + 2] += v[2]; r[4 * g + 3] += v[3]; }
    u32x4 sd[2]; epi_qk(r, 1.f, sd);
    const rsrc_t ro = mk_rsrc(out, SEQ * DM * 2); const unsigned oo = ((rb * 64 + (w >> 1) * 32 + r32) * DM + nb * 64 + (w & 1) * 32 + 8 * hh) * 2;
    __builtin_amdgcn_raw_buffer_store_b128(sd[0], ro, oo, 0, 0); __builtin_amdgcn_raw_buffer_store_b128(sd[1], ro, oo + 32, 0, 0);
}

__global__ __launch_bounds__(512, 2) void fused(const uint16_t* x, const uint16_t* wqkv, const uint16_t* wo, const uint16_t* wg, const uint16_t* wu,
                                                const uint16_t* wd, const uint16_t* gw, char* ws, uint16_t* out) {
    extern __shared__ __attribute__((aligned(16))) char smem[];
    lptr lds = (lptr)smem;
    u64 pc; asm volatile("s_getpc_b64 %0" : "=s"(pc));
    const int tid = threadIdx.x, lane = tid & 63; const unsigned wid = rfl(tid >> 6);
    const unsigned b = blockIdx.x, xc = b & 7, j = b >> 3;
    STAMP_DECL; STAMP(0);
    u64 tag = *(const __attribute__((address_space(4))) u64*)(ws + WS_E);
    tag += 0x9E3779B97F4A7C15ull; tag = (tag ^ (tag >> 30)) * 0xBF58476D1CE4E5B9ull; tag = (tag ^ (tag >> 27)) * 0x94D049BB133111EBull; tag ^= tag >> 31;
    const rsrc_t rws = mk_rsrc(ws, WS_END);
    OutC oc; oc.rd = mk_rsrc(wd, FF * DM * 2); oc.ro = mk_rsrc(wo, DM * DM * 2); oc.rws = rws; oc.w = wid & 3; oc.rb = xc * 4 + (j & 3);
    oc.l0 = (unsigned)(uintptr_t)lds + oc.w * 4096; oc.va = lane * 16;
    oc.vw = j < 28 ? (lane >> 3) * (DM * 2) + (j >> 2) * 128 + (((lane & 7) ^ (((lane >> 4) & 1) << 2)) << 4) : OOB;
    const unsigned rb = 8 * (xc & 1) + (j & 7), cg = 4 * (j >> 3) + (xc >> 1);
    const unsigned rp = wid & 1, pi = wid >> 1, rt = wid & 3, half = wid >> 2;
    const unsigned h = 2 * xc + (j & 1), ab = j >> 1;
    P1 c; c.lds = lds; c.l0 = (unsigned)(uintptr_t)lds + wid * 2048; c.lane = lane; c.oc = oc; c.rws = rws;
    {
        const unsigned xr = 16 * wid + (lane >> 3), qb = cg < 7 ? 14 + cg : cg - 7;
        c.sx.r = mk_rsrc(x, SEQ * DM * 2); c.sx.step = 128;
        c.sx.v0 = (rb * 128 + xr) * (DM * 2) + (((lane & 7) ^ ((xr >> 1) & 7)) << 4); c.sx.v1 = (rb * 128 + xr + 8) * (DM * 2) + (((lane & 7) ^ (((xr + 8) >> 1) & 7)) << 4);
        issue2<0>(c);
        const bool ff = cg < 14; const unsigned pitch = ff ? FF * 2 : 3 * DM * 2;
        c.sf = mk_wsrc(mk_rsrc(ff ? ((wid & 2) ? wu : wg) : wqkv, DM * pitch), pitch, (ff ? 2 * cg + half : 4 * cg - 49 + pi) * 128, 16 * rp, 32, lane);
        if (cg == 15 && pi == 3) {
            const unsigned sc = (lane & 7) ^ (((lane >> 4) & 1) << 2), r = 16 * rp + (lane >> 3);
            c.sf.r = mk_rsrc(gw, DM * 2); c.sf.step = 64; c.sf.v0 = sc == (r & 1) ? (r & ~1u) * 2 : OOB; c.sf.v1 = c.sf.v0 + 16;
        }
        issue2<20>(c);
        __builtin_amdgcn_s_barrier();
        issue2<21>(c); issue2<1>(c);
        {
            dma4(c.sf.r, lane < 20 ? (128 + ((j & 7) * 2 + rp) * 20 + lane) * pitch + (ff ? 2 * cg + half : 4 * cg - 49 + pi) * 128 : OOB, c.l0 + it_reg(0, 22));
            if (NTOUCH > 1) dma4(mk_rsrc((const void*)pc, CODE_PF), lane < 2 ? (lane * 256 + j * 8 + wid) * 128 : OOB, c.l0 + it_reg(0, 22));
        }
        c.sq = mk_wsrc(mk_rsrc(wqkv, ff ? DM * 3 * DM * 2 : 0), 3 * DM * 2, qb * 128, 16 * wid, 128, lane);
        const unsigned r32 = lane & 31, hh = lane >> 5, q = (lane & 15) >> 2, wl = (8 * hh + q) * 128 + ((lane >> 4) & 1) * 32 + (lane & 3) * 8;
        c.xsw = hh ^ ((r32 >> 1) & 7);
#pragma unroll
        for (int u = 0; u < 3; ++u) {
            c.xf[u] = u * 65536 + (64 * rp + r32) * 128; c.xq[u] = u * 65536 + (32 * rt + r32) * 128;
            c.wf[u] = u * 65536 + half * 8192 + wl + (((pi & 1) ^ (q >> 1)) << 6); c.wq[u] = u * 65536 + wl + ((half ^ (q >> 1)) << 6);
            PIN(c.xf[u]); PIN(c.xq[u]); PIN(c.wf[u]); PIN(c.wq[u]);
        }
    }
    asm volatile("" : "+s"(tag), "+s"(out)); c.tag = tag;
    f32x16 acc[4];
    const unsigned fh = cg < 14 ? WS_FH + (rb * 16 + cg) * 8 : OOB, ho = cg < 14 ? WS_H + ((rb * 4 + 2 * rp) * 112 + (cg * 4 + pi) * 2) * 1024 + lane * 16 : OOB;
    if (wid < 4) {
        const unsigned hb = (28 * h + ((lane & 4) ? 27 : 0)) >> 6, grp = (lane >> 3) & 3;
        c.pollo = WS_FQ + ((grp == 3 ? hb : (grp == 1 ? 14 + hb : 7 + hb)) * 16 + (grp < 2 ? rt * 4 + (lane & 3) : (grp == 2 ? 0 : ab))) * 8;
        c.fqo = (tid == 0 && cg < 14) ? WS_FQ + ((cg < 7 ? 14 + cg : cg - 7) * 16 + rb) * 8 : OOB;
        phase1<0>(c, acc, cg, rb, wid STAMP_PASS);
        STAMP(2);
        attn_part(ws, rws, lds, wid, lane, h, ab, tag, fh, ho, acc, __all((c.fq[0] == (unsigned)tag) & (c.fq[1] == (unsigned)(tag >> 32))), c.pollo STAMP_PASS);
        STAMP(5);
        STAMP_DUMP();
        __builtin_amdgcn_endpgm();
    }
    {
        phase1<1>(c, acc, cg, rb, wid STAMP_PASS);
        if (cg < 14) {
            u32x4 hd[4]; epi_h(acc[0], acc[1], hd); epi_h(acc[2], acc[3], hd + 2);
            PIN(hd[0]); PIN(hd[1]); PIN(hd[2]); PIN(hd[3]);
            if (BWAIT) lds_wait(lds + L_CNT + 32 + rt * 4, 1);
            store_h(hd, ho, rws);
        }
        __builtin_amdgcn_s_setprio(3);
        waitv<0>(); signal_last(lds + L_CNT + 64, rws, fh, tag, lane);
        STAMP(3);
        if (j < 28) out_part(oc, ws, out, lds, lane, j >> 2, tag STAMP_PASS);
        else if (b == 255 && wid == 4) {
            wait_flags<6, 20>(rws, WS_FQ + lane * 8, tag, 21 * 128);
            set_flag(rws, lane == 0 ? WS_E : OOB, tag);
        }
        STAMP(7);
        STAMP_DUMP();
        __builtin_amdgcn_endpgm();
    }
}

extern "C" void kernel_launch(void* const* d_in, const int* in_sizes, int n_in, void* d_out, int out_size, void* d_ws, size_t ws_size, hipStream_t stream) {
    (void)hipFuncSetAttribute((const void*)fused, hipFuncAttributeMaxDynamicSharedMemorySize, LDS_BYTES);
    fused<<<256, 512, LDS_BYTES, stream>>>((const uint16_t*)d_in[0], (const uint16_t*)d_in[1], (const uint16_t*)d_in[2], (const uint16_t*)d_in[4], (const uint16_t*)d_in[5],
                                           (const uint16_t*)d_in[6], (const uint16_t*)d_in[7], (char*)d_ws, (uint16_t*)d_out);
}
```

```cpp
#include <hip/hip_runtime.h>
#include <hip/hip_bf16.h>
#include <stdint.h>

typedef short bf16x8 __attribute__((ext_vector_type(8)));
typedef short s16x4 __attribute__((ext_vector_type(4)));
typedef float f32x16 __attribute__((ext_vector_type(16)));
typedef float f32x4 __attribute__((ext_vector_type(4)));
typedef unsigned u32x4 __attribute__((ext_vector_type(4)));
typedef unsigned u32x2 __attribute__((ext_vector_type(2)));
typedef __amdgpu_buffer_rsrc_t rsrc_t;
#define LAS __attribute__((address_space(3)))
#define DI __device__ __forceinline__
typedef LAS char* lptr;

constexpr int SEQ = 2048, DM = 448, NH = 16, DH = 28, FF = 1792;
constexpr float QSCALE = 0.18898223650461362f * 1.4426950408889634f;
constexpr float LOG2E = 1.4426950408889634f;

constexpr size_t WS_Q = 0;
constexpr size_t WS_KV = WS_Q + (2u << 20);
constexpr size_t WS_O = WS_KV + (4u << 20);
constexpr size_t WS_H = WS_O + (2u << 20);
constexpr size_t WS_G = WS_H + (size_t)64 * 112 * 1024;
constexpr int LDS_BYTES = 80 * 1024;

#define MFMA(a, b, c) __builtin_amdgcn_mfma_f32_32x32x16_bf16((a), (b), (c), 0, 0, 0)
#define SBAR() __builtin_amdgcn_sched_barrier(0)
#define WAITV(n) asm volatile("s_waitcnt vmcnt(" #n ")" ::: "memory")
#define BARRIER() asm volatile("s_waitcnt lgkmcnt(0)\n\ts_barrier" ::: "memory")

#define STAMP(kern, i) do {} while (0)
DI rsrc_t mk_rsrc(const void* p, unsigned bytes) { return __builtin_amdgcn_make_buffer_rsrc((void*)p, (short)0, (int)bytes, 0x00020000); }
DI unsigned rfl(unsigned v) { return (unsigned)__builtin_amdgcn_readfirstlane((int)v); }
DI void dma16(rsrc_t r, unsigned voff, unsigned soff, unsigned ldsaddr) {
    asm volatile("s_mov_b32 m0, %0\n\ts_nop 0\n\tbuffer_load_dwordx4 %1, %2, %3 offen lds" :: "s"(ldsaddr), "v"(voff), "s"(r), "s"(soff) : "memory");
}
DI void gload16(u32x4& dst, rsrc_t r, unsigned voff, unsigned soff) {
    asm volatile("buffer_load_dwordx4 %0, %1, %2, %3 offen" : "=v"(dst) : "v"(voff), "s"(r), "s"(soff) : "memory");
}
typedef float f32x2 __attribute__((ext_vector_type(2)));
typedef __bf16 bf16x2_t __attribute__((ext_vector_type(2)));
DI unsigned cvtpk(float lo, float hi) { return __builtin_bit_cast(unsigned, __builtin_convertvector((f32x2){lo, hi}, bf16x2_t)); }
DI bf16x8 lds_b128(lptr p) { return *(const LAS bf16x8*)p; }
typedef short v4i16_t __attribute__((ext_vector_type(4)));
DI s16x4 lds_tr(lptr p) { return __builtin_bit_cast(s16x4, __builtin_amdgcn_ds_read_tr16_b64_v4i16((LAS v4i16_t*)p)); }
DI bf16x8 cat(s16x4 lo, s16x4 hi) { return __builtin_shufflevector(lo, hi, 0, 1, 2, 3, 4, 5, 6, 7); }
DI u32x4 pack8(const f32x16& a, int s) {
    u32x4 r; r[0] = cvtpk(a[8 * s], a[8 * s + 1]); r[1] = cvtpk(a[8 * s + 2], a[8 * s + 3]); r[2] = cvtpk(a[8 * s + 4], a[8 * s + 5]); r[3] = cvtpk(a[8 * s + 6], a[8 * s + 7]); return r;
}
DI float bf2f(unsigned short v) { return __uint_as_float((unsigned)v << 16); }

DI void qkv_part(const uint16_t* x, const uint16_t* wqkv, char* ws, lptr lds, int h, int rb) {
    const int tid = threadIdx.x, lane = tid & 63, r32 = lane & 31, hh = lane >> 5; const unsigned wid = rfl(tid >> 6);
    const rsrc_t rx = mk_rsrc(x, SEQ * DM * 2), rw = mk_rsrc(wqkv, DM * 3 * DM * 2);
    constexpr unsigned XB = 16384, STG = 28672;
    const unsigned l0 = (unsigned)(uintptr_t)lds;
    unsigned vx[4], vw[3];
#pragma unroll
    for (int p = 0; p < 4; ++p) { const unsigned r = 32 * wid + 8 * p + (lane >> 3); vx[p] = (rb * 128 + r) * (DM * 2) + (((lane & 7) ^ ((r >> 1) & 7)) << 4); }
#pragma unroll
    for (int j = 0; j < 3; ++j) vw[j] = (16 * wid + (lane >> 2)) * (3 * DM * 2) + (DM * j + DH * h) * 2 + ((lane & 3) << 4);
    auto stage = [&](int kt) {
        const unsigned sl = l0 + (kt & 1) * STG;
#pragma unroll
        for (int p = 0; p < 4; ++p) dma16(rx, vx[p], kt * 128, sl + wid * 4096 + p * 1024);
#pragma unroll
        for (int j = 0; j < 3; ++j) dma16(rw, vw[j], kt * 64 * (3 * DM * 2), sl + XB + j * 4096 + wid * 1024);
    };
    stage(0); stage(1);
    f32x16 aq = {}, ak = {}, av = {};
    const unsigned xoff = (32 * wid + r32) * 128, xsw = (r32 >> 1) & 7;
    const unsigned woff = XB + (8 * hh + ((lane & 15) >> 2)) * 64 + ((lane >> 4) & 1) * 32 + (lane & 3) * 8;
#pragma unroll 1
    for (int kt = 0; kt < 7; ++kt) {
        if (kt < 6) WAITV(7); else WAITV(0);
        BARRIER();
        lptr sl = lds + (kt & 1) * STG;
#pragma unroll
        for (int s = 0; s < 4; ++s) {
            const bf16x8 xf = lds_b128(sl + xoff + (((2 * s + hh) ^ xsw) << 4));
            const bf16x8 wq = cat(lds_tr(sl + woff + s * 1024), lds_tr(sl + woff + s * 1024 + 256));
            const bf16x8 wk = cat(lds_tr(sl + woff + 4096 + s * 1024), lds_tr(sl + woff + 4096 + s * 1024 + 256));
            const bf16x8 wv = cat(lds_tr(sl + woff + 8192 + s * 1024), lds_tr(sl + woff + 8192 + s * 1024 + 256));
            aq = MFMA(wq, xf, aq); ak = MFMA(wk, xf, ak); av = MFMA(xf, wv, av);
        }
        BARRIER();
        if (kt + 2 < 7) stage(kt + 2);
    }
#pragma unroll
    for (int i = 0; i < 16; ++i) aq[i] *= QSCALE;
    if (hh) {
#pragma unroll
        for (int i = 12; i < 16; ++i) { aq[i] = 0.f; ak[i] = 0.f; }
    }
    if (r32 >= DH) { const float c = r32 == DH ? 1.f : 0.f;
#pragma unroll
        for (int i = 0; i < 16; ++i) av[i] = c; }
    const int tt = rb * 4 + wid;
    u32x4* qd = (u32x4*)(ws + WS_Q + ((size_t)(h * 64 + tt) * 2048)) + lane;
    u32x4* kd = (u32x4*)(ws + WS_KV + ((size_t)(h * 64 + tt) * 4096)) + lane;
    qd[0] = pack8(aq, 0); qd[64] = pack8(aq, 1);
    kd[0] = pack8(ak, 0); kd[64] = pack8(ak, 1); kd[128] = pack8(av, 0); kd[192] = pack8(av, 1);
}

DI void glu_part(const uint16_t* x, const uint16_t* wg, const uint16_t* wu, char* ws, lptr lds, int rb, int cg) {
    const int tid = threadIdx.x, lane = tid & 63, r32 = lane & 31, hh = lane >> 5; const unsigned wid = rfl(tid >> 6);
    const rsrc_t rx = mk_rsrc(x, SEQ * DM * 2), rg = mk_rsrc(wg, DM * FF * 2), ru = mk_rsrc(wu, DM * FF * 2);
    constexpr unsigned STG = 24576;
    const unsigned l0 = (unsigned)(uintptr_t)lds;
    unsigned vx[2], vw[2];
#pragma unroll
    for (int p = 0; p < 2; ++p) {
        const unsigned r = 16 * (2 * wid + p) + (lane >> 2); vx[p] = (rb * 128 + r) * (DM * 2) + (((lane & 3) ^ ((r >> 2) & 3)) << 4);
        const unsigned k = 4 * (2 * wid + p) + (lane >> 4); vw[p] = k * (FF * 2) + cg * 256 + (((lane & 15) ^ ((k & 3) << 2)) << 4);
    }
    auto stage = [&](int kt, unsigned slot) {
        const unsigned sl = l0 + slot;
#pragma unroll
        for (int p = 0; p < 2; ++p) {
            dma16(rx, vx[p], kt * 64, sl + wid * 2048 + p * 1024);
            dma16(rg, vw[p], kt * 32 * (FF * 2), sl + 8192 + wid * 2048 + p * 1024);
            dma16(ru, vw[p], kt * 32 * (FF * 2), sl + 16384 + wid * 2048 + p * 1024);
        }
    };
    stage(0, 0); stage(1, STG);
    const unsigned wr = wid & 1, wp = wid >> 1;
    f32x16 ag[2][2] = {}, au[2][2] = {};
    const unsigned q = (lane & 15) >> 2;
    const unsigned xoff = (64 * wr + r32) * 64, xsw = (r32 >> 2) & 3;
    unsigned woff[2];
#pragma unroll
    for (int c = 0; c < 2; ++c) woff[c] = 8192 + (8 * hh + q) * 256 + (((2 * wp + c) ^ q) << 6) + ((lane >> 4) & 1) * 32 + (lane & 3) * 8;
    unsigned cur = 0, nxt2 = 2 * STG;
#pragma unroll 1
    for (int kt = 0; kt < 14; ++kt) {
        if (kt < 13) WAITV(6); else WAITV(0);
        BARRIER();
        if (kt + 2 < 14) stage(kt + 2, nxt2);
        lptr sl = lds + cur;
#pragma unroll
        for (int s = 0; s < 2; ++s) {
            bf16x8 xf[2], gf[2], uf[2];
#pragma unroll
            for (int t = 0; t < 2; ++t) xf[t] = lds_b128(sl + xoff + t * 2048 + (((2 * s + hh) ^ xsw) << 4));
#pragma unroll
            for (int c = 0; c < 2; ++c) {
                gf[c] = cat(lds_tr(sl + woff[c] + s * 4096), lds_tr(sl + woff[c] + s * 4096 + 1024));
                uf[c] = cat(lds_tr(sl + woff[c] + 8192 + s * 4096), lds_tr(sl + woff[c] + 8192 + s * 4096 + 1024));
            }
#pragma unroll
            for (int t = 0; t < 2; ++t)
#pragma unroll
                for (int c = 0; c < 2; ++c) { ag[t][c] = MFMA(gf[c], xf[t], ag[t][c]); au[t][c] = MFMA(uf[c], xf[t], au[t][c]); }
        }
        nxt2 = cur; cur = (cur == 2 * STG) ? 0 : cur + STG;
    }
#pragma unroll
    for (int t = 0; t < 2; ++t)
#pragma unroll
        for (int c = 0; c < 2; ++c) {
            f32x16 hv;
#pragma unroll
            for (int i = 0; i < 16; ++i) hv[i] = au[t][c][i] * __builtin_amdgcn_rcpf(1.f + __builtin_amdgcn_exp2f(-LOG2E * ag[t][c][i]));
            const int tt = rb * 4 + 2 * wr + t, ks = (cg * 4 + 2 * wp + c) * 2;
            u32x4* hd = (u32x4*)(ws + WS_H + ((size_t)(tt * 112 + ks) * 1024)) + lane;
            hd[0] = pack8(hv, 0); hd[64] = pack8(hv, 1);
        }
}

DI void gate_part(const uint16_t* x, const uint16_t* gw, char* ws, int blk) {
    const int tid = threadIdx.x, row = blk * 64 + (tid >> 2), part = tid & 3;
    float s = 0.f;
    const u32x4* xr = (const u32x4*)(x + (size_t)row * DM); const u32x4* gr = (const u32x4*)gw;
#pragma unroll
    for (int i = 0; i < 14; ++i) {
        const u32x4 a = xr[i * 4 + part], b = gr[i * 4 + part];
#pragma unroll
        for (int j = 0; j < 4; ++j) {
            s += __uint_as_float(a[j] << 16) * __uint_as_float(b[j] << 16);
            s += __uint_as_float(a[j] & 0xffff0000u) * __uint_as_float(b[j] & 0xffff0000u);
        }
    }
    s += __shfl_xor(s, 1); s += __shfl_xor(s, 2);
    if (part == 0) ((float*)(ws + WS_G))[row] = __builtin_amdgcn_rcpf(1.f + __builtin_amdgcn_exp2f(-LOG2E * s));
}

DI float xhalf_max(float m) { auto rr = __builtin_amdgcn_permlane32_swap(__float_as_uint(m), __float_as_uint(m), false, false); return fmaxf(__uint_as_float(rr[0]), __uint_as_float(rr[1])); }
DI float max16(const f32x16& p) {
    float a = fmaxf(p[0], p[1]);
#pragma unroll
    for (int i = 2; i < 16; ++i) a = fmaxf(a, p[i]);
    return xhalf_max(a);
}
template <bool SAFE>
DI void attn_pass(char* ws, lptr lds, int h, const bf16x8 q0, const bf16x8 q1, f32x16& o) {
    const int tid = threadIdx.x, lane = tid & 63; const unsigned wid = rfl(tid >> 6);
    const rsrc_t rkv = mk_rsrc(ws + WS_KV + (size_t)h * 64 * 4096, 64 * 4096);
    const unsigned l0 = (unsigned)(uintptr_t)lds;
    const unsigned vsrc = wid * 4096 + lane * 16;
    auto stage = [&](int c) {
#pragma unroll
        for (int p = 0; p < 4; ++p) dma16(rkv, vsrc, c * 16384 + p * 1024, l0 + (c & 3) * 16384 + wid * 4096 + p * 1024);
    };
    stage(0); stage(1); stage(2);
    WAITV(8); BARRIER();
    lptr lp = lds + lane * 16;
    f32x16 zero = {};
    o = zero;
    if constexpr (!SAFE) {
        bf16x8 k0 = lds_b128(lp), k1 = lds_b128(lp + 1024);
        f32x16 sA = MFMA(k0, q0, zero); sA = MFMA(k1, q1, sA);
        const float nm = -max16(sA);
        f32x16 cm;
#pragma unroll
        for (int i = 0; i < 16; ++i) { cm[i] = nm; sA[i] += nm; }
        f32x16 sB;
        k0 = lds_b128(lp + 4096); k1 = lds_b128(lp + 4096 + 1024);
        bf16x8 v0 = lds_b128(lp + 2048), v1 = lds_b128(lp + 3072);
#define ATT_STEP(SC, SN, KOFF, VOFF) do { \
        SN = MFMA(k0, q0, cm); SN = MFMA(k1, q1, SN); \
        bf16x8 kn0 = lds_b128(lp + (KOFF)), kn1 = lds_b128(lp + (KOFF) + 1024); \
        bf16x8 vn0 = lds_b128(lp + (VOFF) + 2048), vn1 = lds_b128(lp + (VOFF) + 3072); \
        _Pragma("unroll") for (int i = 0; i < 16; ++i) SC[i] = __builtin_amdgcn_exp2f(SC[i]); \
        o = MFMA(v0, __builtin_bit_cast(bf16x8, pack8(SC, 0)), o); \
        o = MFMA(v1, __builtin_bit_cast(bf16x8, pack8(SC, 1)), o); \
        k0 = kn0; k1 = kn1; v0 = vn0; v1 = vn1; } while (0)
#pragma unroll 1
        for (int c = 0; c < 16; ++c) {
            const unsigned cb = (c & 3) * 16384, nb = ((c + 1) & 3) * 16384;
            ATT_STEP(sA, sB, cb + 2 * 4096, cb + 1 * 4096);
            ATT_STEP(sB, sA, cb + 3 * 4096, cb + 2 * 4096);
            if (c < 14) WAITV(4); else WAITV(0);
            BARRIER();
            if (c + 3 < 16) stage(c + 3);
            ATT_STEP(sA, sB, nb, cb + 3 * 4096);
            ATT_STEP(sB, sA, nb + 4096, nb);
        }
#undef ATT_STEP
    } else {
        float m = -INFINITY;
#pragma unroll 1
        for (int c = 0; c < 16; ++c) {
            const unsigned cb = (c & 3) * 16384;
#pragma unroll 1
            for (int t = 0; t < 4; ++t) {
                lptr tp = lp + cb + t * 4096;
                f32x16 s = MFMA(lds_b128(tp), q0, zero); s = MFMA(lds_b128(tp + 1024), q1, s);
                const float mn = fmaxf(m, max16(s)); const float al = __builtin_amdgcn_exp2f(m - mn); m = mn;
#pragma unroll
                for (int i = 0; i < 16; ++i) { s[i] = __builtin_amdgcn_exp2f(s[i] - mn); o[i] *= al; }
                o = MFMA(lds_b128(tp + 2048), __builtin_bit_cast(bf16x8, pack8(s, 0)), o);
                o = MFMA(lds_b128(tp + 3072), __builtin_bit_cast(bf16x8, pack8(s, 1)), o);
            }
            if (c < 14) WAITV(4); else WAITV(0);
            BARRIER();
            if (c + 3 < 16) stage(c + 3);
        }
    }
    BARRIER();
}
DI void attn_part(char* ws, lptr lds, int h, int rb) {
    const int tid = threadIdx.x, lane = tid & 63, hh = lane >> 5; const unsigned wid = rfl(tid >> 6);
    const int tt = rb * 4 + wid;
    const bf16x8* qs = (const bf16x8*)(ws + WS_Q + ((size_t)(h * 64 + tt) * 2048)) + lane;
    const bf16x8 q0 = qs[0], q1 = qs[64];
    f32x16 o;
    attn_pass<false>(ws, lds, h, q0, q1, o);
    float mag = 0.f;
#pragma unroll
    for (int i = 0; i < 16; ++i) mag = fmaxf(mag, fabsf(o[i]));
    bool bad = !(mag < 1.8e19f);
#pragma unroll
    for (int i = 0; i < 16; ++i) bad |= (o[i] != o[i]);
    if (__syncthreads_or(bad)) attn_pass<true>(ws, lds, h, q0, q1, o);
    auto rr = __builtin_amdgcn_permlane32_swap(__float_as_uint(o[12]), __float_as_uint(o[12]), false, false);
    const float inv = __builtin_amdgcn_rcpf(__uint_as_float(rr[1]));
#pragma unroll
    for (int i = 0; i < 16; ++i) o[i] *= inv;
    if (hh) {
#pragma unroll
        for (int i = 12; i < 16; ++i) o[i] = 0.f;
    }
    u32x4* od = (u32x4*)(ws + WS_O + ((size_t)(tt * 16 + h) * 2048)) + lane;
    od[0] = pack8(o, 0); od[64] = pack8(o, 1);
}

DI void out_part(const uint16_t* wd, const uint16_t* wo, char* ws, uint16_t* out, lptr lds, int rb, int nb) {
    const int tid = threadIdx.x, lane = tid & 63, r32 = lane & 31, hh = lane >> 5; const unsigned wid = rfl(tid >> 6);
    const rsrc_t rd = mk_rsrc(wd, FF * DM * 2), ro = mk_rsrc(wo, DM * DM * 2);
    const rsrc_t rh = mk_rsrc(ws + WS_H, 64 * 112 * 1024), rof = mk_rsrc(ws + WS_O, 2u << 20);
    const unsigned l0 = (unsigned)(uintptr_t)lds + wid * 20480;
    const unsigned vw = (lane >> 3) * (DM * 2) + nb * 128 + (((lane & 7) ^ (((lane >> 4) & 1) << 2)) << 4);
    const unsigned va = lane * 16;
    constexpr int P = 4, NS = 5, NST = 18;
    u32x4 af[P][4];
    auto issue = [&](int i) {
        const unsigned dst = l0 + (i % NS) * 4096;
        if (i < 14) {
            const unsigned so = (wid * 448 + i * 32) * (DM * 2);
#pragma unroll
            for (int p = 0; p < 4; ++p) dma16(rd, vw, so + p * 8 * (DM * 2), dst + p * 1024);
#pragma unroll
            for (int t = 0; t < 2; ++t)
#pragma unroll
                for (int s = 0; s < 2; ++s) gload16(af[i % P][2 * t + s], rh, va, ((rb * 2 + t) * 112 + wid * 28 + 2 * i + s) * 1024);
        } else {
            const unsigned hd = wid * 4 + (i - 14), so = hd * DH * (DM * 2);
#pragma unroll
            for (int p = 0; p < 4; ++p) dma16(ro, vw, so + p * 8 * (DM * 2), dst + p * 1024);
#pragma unroll
            for (int t = 0; t < 2; ++t)
#pragma unroll
                for (int s = 0; s < 2; ++s) gload16(af[i % P][2 * t + s], rof, va, (((rb * 2 + t) * 16 + hd) * 2 + s) * 1024);
        }
    };
#pragma unroll
    for (int i = 0; i < P; ++i) issue(i);
    f32x16 ab[2][2] = {}, aa[2][2] = {};
    const unsigned q = (lane & 15) >> 2;
    unsigned woff[2];
#pragma unroll
    for (int c = 0; c < 2; ++c) woff[c] = wid * 20480 + (4 * hh + q) * 128 + ((c ^ (q >> 1)) << 6) + ((lane >> 4) & 1) * 32 + (lane & 3) * 8;
#pragma unroll
    for (int i = 0; i < NST; ++i) {
        u32x4(&a)[4] = af[i % P];
        const int rem = NST - 1 - i < P - 1 ? NST - 1 - i : P - 1;
        if (rem == 3) asm volatile("s_waitcnt vmcnt(24)" : "+v"(a[0]), "+v"(a[1]), "+v"(a[2]), "+v"(a[3]) :: "memory");
        else if (rem == 2) asm volatile("s_waitcnt vmcnt(16)" : "+v"(a[0]), "+v"(a[1]), "+v"(a[2]), "+v"(a[3]) :: "memory");
        else if (rem == 1) asm volatile("s_waitcnt vmcnt(8)" : "+v"(a[0]), "+v"(a[1]), "+v"(a[2]), "+v"(a[3]) :: "memory");
        else asm volatile("s_waitcnt vmcnt(0)" : "+v"(a[0]), "+v"(a[1]), "+v"(a[2]), "+v"(a[3]) :: "memory");
        lptr sl = lds + (i % NS) * 4096;
#pragma unroll
        for (int s = 0; s < 2; ++s) {
            bf16x8 wf[2];
#pragma unroll
            for (int c = 0; c < 2; ++c) wf[c] = cat(lds_tr(sl + woff[c] + s * 2048), lds_tr(sl + woff[c] + s * 2048 + 1024));
#pragma unroll
            for (int t = 0; t < 2; ++t)
#pragma unroll
                for (int c = 0; c < 2; ++c) {
                    if (i < 14) ab[t][c] = MFMA(wf[c], __builtin_bit_cast(bf16x8, a[2 * t + s]), ab[t][c]);
                    else aa[t][c] = MFMA(wf[c], __builtin_bit_cast(bf16x8, a[2 * t + s]), aa[t][c]);
                }
        }
        SBAR();
        if (i + P < NST) issue(i + P);
    }
    const float* G = (const float*)(ws + WS_G);
#pragma unroll
    for (int t = 0; t < 2; ++t) { const float g = G[rb * 64 + t * 32 + r32], g1 = 1.f - g;
#pragma unroll
        for (int c = 0; c < 2; ++c)
#pragma unroll
            for (int i = 0; i < 16; ++i) ab[t][c][i] = g * aa[t][c][i] + g1 * ab[t][c][i]; }
    BARRIER();
#pragma unroll
    for (int t = 0; t < 2; ++t)
#pragma unroll
        for (int c = 0; c < 2; ++c)
#pragma unroll
            for (int g = 0; g < 4; ++g)
                *(LAS f32x4*)(lds + (((2 * t + c) * 4 + wid) * 4 + g) * 1024 + lane * 16) = (f32x4){ab[t][c][4 * g], ab[t][c][4 * g + 1], ab[t][c][4 * g + 2], ab[t][c][4 * g + 3]};
    BARRIER();
    const unsigned t = wid >> 1, c = wid & 1;
    uint16_t* orow = out + (size_t)(rb * 64 + t * 32 + r32) * DM + nb * 64 + c * 32 + 4 * hh;
#pragma unroll
    for (int g = 0; g < 4; ++g) {
        f32x4 s = *(LAS f32x4*)(lds + ((wid * 4 + 0) * 4 + g) * 1024 + lane * 16);
#pragma unroll
        for (int w = 1; w < 4; ++w) s += *(LAS f32x4*)(lds + ((wid * 4 + w) * 4 + g) * 1024 + lane * 16);
        *(u32x2*)(orow + 8 * g) = (u32x2){cvtpk(s[0], s[1]), cvtpk(s[2], s[3])};
    }
}

__global__ __launch_bounds__(256, 2) void k1(const uint16_t* x, const uint16_t* wqkv, const uint16_t* wg, const uint16_t* wu, const uint16_t* gw, char* ws) {
    extern __shared__ __attribute__((aligned(16))) char smem[];
    const int b = blockIdx.x;
    STAMP(0, 0);
    if (b < 256) qkv_part(x, wqkv, ws, (lptr)smem, 2 * (b & 7) + ((b >> 3) & 1), b >> 4);
    else if (b < 480) { const int i = b - 256, xc = i & 7, j = i >> 3;
        glu_part(x, wg, wu, ws, (lptr)smem, (xc >> 1) * 4 + (j & 3), (xc & 1) * 7 + (j >> 2)); }
    else gate_part(x, gw, ws, b - 480);
    STAMP(0, 1);
}
__global__ __launch_bounds__(256, 2) void k2(char* ws) {
    extern __shared__ __attribute__((aligned(16))) char smem[];
    const int b = blockIdx.x;
    STAMP(1, 0);
    attn_part(ws, (lptr)smem, 2 * (b & 7) + ((b >> 3) & 1), b >> 4);
    STAMP(1, 1);
}
__global__ __launch_bounds__(256, 2) void k3(const uint16_t* wd, const uint16_t* wo, char* ws, uint16_t* out) {
    extern __shared__ __attribute__((aligned(16))) char smem[];
    const int b = blockIdx.x, xc = b & 7, j = b >> 3;
    STAMP(2, 0);
    out_part(wd, wo, ws, out, (lptr)smem, xc * 4 + (j & 3), j >> 2);
    STAMP(2, 1);
}

extern "C" void kernel_launch(void* const* d_in, const int* in_sizes, int n_in, void* d_out, int out_size, void* d_ws, size_t ws_size, hipStream_t stream) {
    const uint16_t* x = (const uint16_t*)d_in[0];
    const uint16_t* wqkv = (const uint16_t*)d_in[1];
    const uint16_t* wo = (const uint16_t*)d_in[2];
    const uint16_t* wg = (const uint16_t*)d_in[4];
    const uint16_t* wu = (const uint16_t*)d_in[5];
    const uint16_t* wd = (const uint16_t*)d_in[6];
    const uint16_t* gw = (const uint16_t*)d_in[7];
    char* ws = (char*)d_ws;
    (void)hipFuncSetAttribute((const void*)k1, hipFuncAttributeMaxDynamicSharedMemorySize, LDS_BYTES);
    (void)hipFuncSetAttribute((const void*)k2, hipFuncAttributeMaxDynamicSharedMemorySize, LDS_BYTES);
    (void)hipFuncSetAttribute((const void*)k3, hipFuncAttributeMaxDynamicSharedMemorySize, LDS_BYTES);
    k1<<<512, 256, LDS_BYTES, stream>>>(x, wqkv, wg, wu, gw, ws);
    k2<<<256, 256, LDS_BYTES, stream>>>(ws);
    k3<<<224, 256, LDS_BYTES, stream>>>(wd, wo, ws, (uint16_t*)d_out);
}
```

```cpp
#include <hip/hip_runtime.h>
#include <hip/hip_bf16.h>
#include <stdint.h>

typedef short bf16x8 __attribute__((ext_vector_type(8)));
typedef short s16x4 __attribute__((ext_vector_type(4)));
typedef float f32x16 __attribute__((ext_vector_type(16)));
typedef float f32x4 __attribute__((ext_vector_type(4)));
typedef float f32x2 __attribute__((ext_vector_type(2)));
typedef unsigned u32x4 __attribute__((ext_vector_type(4)));
typedef unsigned u32x2 __attribute__((ext_vector_type(2)));
typedef __bf16 bf16x2_t __attribute__((ext_vector_type(2)));
typedef unsigned long long u64;
typedef __amdgpu_buffer_rsrc_t rsrc_t;
#define LAS __attribute__((address_space(3)))
#define DI __device__ __forceinline__
typedef LAS char* lptr;

constexpr int SEQ = 2048, DM = 448, DH = 28, FF = 1792;
constexpr float LOG2E = 1.4426950408889634f;
constexpr float QSCALE = 0.18898223650461362f * LOG2E;

constexpr unsigned WS_Q = 0;
constexpr unsigned WS_K = WS_Q + (2u << 20);
constexpr unsigned WS_V = WS_K + (2u << 20);
constexpr unsigned WS_O = WS_V + (2u << 20);
constexpr unsigned WS_H = WS_O + (2u << 20);
constexpr unsigned WS_G = WS_H + 64u * 112 * 1024;
constexpr unsigned WS_FQ = WS_G + 16384;
constexpr unsigned WS_FH = WS_FQ + 8192;
constexpr unsigned WS_FO = WS_FH + 16384;
constexpr unsigned WS_E = WS_FO + 8192;
constexpr unsigned WS_END = WS_E + 256;
constexpr unsigned CODE_PF = 50000;
constexpr int PSLEEP = 10, PAUSE = 7, NTOUCH = 2;
constexpr unsigned OOB = 0x7ffffff0u;

constexpr int LDS_BYTES = 160 * 1024;
constexpr unsigned RG(int i) { const int t[6] = {4, 0, 1, 3, 7, 2}; return t[i] * 16384; }
#define COMB(w) (((0x8596u >> (4 * (w))) & 15u) << 14)
constexpr unsigned L_CNT = 6 * 16384 + 12288;

#define MFMA(a, b, c) __builtin_amdgcn_mfma_f32_32x32x16_bf16((a), (b), (c), 0, 0, 0)
#define SBAR() __builtin_amdgcn_sched_barrier(0)
#define PIN(x) asm volatile("" : "+v"(x))
#define BARRIER() asm volatile("s_waitcnt lgkmcnt(0)\n\ts_barrier" ::: "memory")
template <int N> DI void waitv() { asm volatile("s_waitcnt vmcnt(%0)" :: "n"(N) : "memory"); }


DI rsrc_t mk_rsrc(const void* p, unsigned bytes) { return __builtin_amdgcn_make_buffer_rsrc((void*)p, (short)0, (int)bytes, 0x00020000); }
DI unsigned rfl(unsigned v) { return (unsigned)__builtin_amdgcn_readfirstlane((int)v); }
DI void dma16(rsrc_t r, unsigned voff, unsigned soff, unsigned ldsaddr) {
    asm volatile("s_mov_b32 m0, %0\n\ts_nop 0\n\tbuffer_load_dwordx4 %1, %2, %3 offen lds" :: "s"(ldsaddr), "v"(voff), "s"(r), "s"(soff) : "memory");
}
DI void dma4(rsrc_t r, unsigned voff, unsigned ldsaddr) {
    asm volatile("s_mov_b32 m0, %0\n\ts_nop 0\n\tbuffer_load_dword %1, %2, 0 offen lds" :: "s"(ldsaddr), "v"(voff), "s"(r) : "memory");
}
DI void gload16_sc1(u32x4& dst, rsrc_t r, unsigned voff, unsigned soff) {
    asm volatile("buffer_load_dwordx4 %0, %1, %2, %3 offen sc1" : "=v"(dst) : "v"(voff), "s"(r), "s"(soff) : "memory");
}
DI bf16x8 ld_sc1(rsrc_t r, unsigned voff, unsigned soff) { return __builtin_bit_cast(bf16x8, __builtin_amdgcn_raw_buffer_load_b128(r, voff, soff, 16)); }
DI void st_sc1(u32x4 v, rsrc_t r, unsigned voff) { __builtin_amdgcn_raw_buffer_store_b128(v, r, voff, 0, 16); }
DI unsigned cvtpk(float lo, float hi) { return __builtin_bit_cast(unsigned, __builtin_convertvector((f32x2){lo, hi}, bf16x2_t)); }
DI bf16x8 lds_b128(lptr p) { return *(const LAS bf16x8*)p; }
DI s16x4 lds_tr(lptr p) { return __builtin_amdgcn_ds_read_tr16_b64_v4i16((LAS s16x4*)p); }
DI bf16x8 cat(s16x4 lo, s16x4 hi) { return __builtin_shufflevector(lo, hi, 0, 1, 2, 3, 4, 5, 6, 7); }
DI u32x4 pack8(const f32x16& a, int s) {
    u32x4 r; r[0] = cvtpk(a[8 * s], a[8 * s + 1]); r[1] = cvtpk(a[8 * s + 2], a[8 * s + 3]); r[2] = cvtpk(a[8 * s + 4], a[8 * s + 5]); r[3] = cvtpk(a[8 * s + 6], a[8 * s + 7]); return r;
}
DI float sigmoidf_(float v) { return __builtin_amdgcn_rcpf(1.f + __builtin_amdgcn_exp2f(-LOG2E * v)); }

DI void set_flag(rsrc_t rws, unsigned off, u64 tag) {
    __builtin_amdgcn_raw_buffer_store_b64((u32x2){(unsigned)tag, (unsigned)(tag >> 32)}, rws, off, 0, 16);
}
template <int NW, int SLEEP = 4> DI void wait_flags(rsrc_t rws, unsigned off, u64 tag, unsigned lim = ~0u) {
#pragma unroll 1
    for (int spin = 0; spin < 40000; ++spin) {
        bool ok = true;
        asm volatile("" ::: "memory");
#pragma unroll
        for (int i = 0; i < NW; ++i) { const u32x2 v = __builtin_amdgcn_raw_buffer_load_b64(rws, off + i * 512, 0, 16); ok &= ((v[0] == (unsigned)tag) & (v[1] == (unsigned)(tag >> 32))) | (off + i * 512 - WS_FQ >= lim); }
        if (__all(ok)) break;
        __builtin_amdgcn_s_sleep(SLEEP);
    }
    asm volatile("" ::: "memory");
}
DI float load_gate(rsrc_t rws, unsigned tok, u64 tag) {
    u32x2 v;
#pragma unroll 1
    for (int spin = 0; spin < 40000; ++spin) {
        asm volatile("" ::: "memory");
        v = __builtin_amdgcn_raw_buffer_load_b64(rws, WS_G + tok * 8, 0, 16);
        if (__all(v[1] == (unsigned)tag)) break;
        __builtin_amdgcn_s_sleep(2);
    }
    return __uint_as_float(v[0]);
}
DI void lds_wait(lptr cnt, unsigned target) {
#pragma unroll 1
    for (int spin = 0; spin < 4000000; ++spin) {
        if (rfl(__hip_atomic_load((LAS unsigned*)cnt, __ATOMIC_RELAXED, __HIP_MEMORY_SCOPE_WORKGROUP)) >= target) break;
        __builtin_amdgcn_s_sleep(4);
    }
    asm volatile("" ::: "memory");
}
DI void group_barrier(lptr words, unsigned w) {
    asm volatile("s_waitcnt lgkmcnt(0)" ::: "memory");
    *(volatile LAS unsigned*)(words + w * 4) = 1u;
#pragma unroll 1
    for (int spin = 0; spin < 4000000; ++spin) {
        const u32x4 v = *(volatile LAS u32x4*)words;
        if (rfl(v[0] & v[1] & v[2] & v[3])) break;
    }
    asm volatile("" ::: "memory");
}

struct WSrc { rsrc_t r; unsigned v0, v1, step; };
DI WSrc mk_wsrc(rsrc_t r, unsigned pitch, unsigned colb, unsigned row0, unsigned rows_per_stage, int lane) {
    WSrc s; s.r = r; const unsigned sw = ((lane & 7) ^ (((lane >> 4) & 1) << 2)) << 4;
    s.v0 = (row0 + (lane >> 3)) * pitch + colb + sw; s.v1 = s.v0 + 8 * pitch; s.step = rows_per_stage * pitch; return s;
}
constexpr int OB_NS = 6, OB_P = 4;
struct OutC { rsrc_t rd, ro, rws; unsigned l0, vw, va, w, rb; };
DI void out_issueW1(const OutC& c, int i, int p) {
    const unsigned so = i < 14 ? (c.w * 448 + i * 32) * (DM * 2) : (c.w * 4 + (i - 14)) * DH * (DM * 2);
    dma16(i < 14 ? c.rd : c.ro, c.vw, so + p * 8 * (DM * 2), c.l0 + RG(i % OB_NS) + p * 1024);
}
DI void out_issueW(const OutC& c, int i) {
#pragma unroll
    for (int p = 0; p < 4; ++p) out_issueW1(c, i, p);
}
constexpr unsigned it_reg(int P, int it) { const int x[7] = {0, 3, 7, 8, 1, 2, 5}, q[4] = {6, 9, 4, 0}, f[14] = {1, 2, 4, 5, 3, 6, 9, 4, 0, 3, 7, 6, 9, 8};
    const int x1[7] = {0, 3, 6, 9, 2, 0, 3}, f1[14] = {1, 2, 4, 5, 7, 8, 1, 4, 5, 7, 8, 1, 4, 5};
    return (P ? (it < 10 ? x1[it] : f1[it - 20]) : (it < 10 ? x[it] : (it < 20 ? q[it - 10] : f[it - 20]))) * 16384; }
constexpr int it_first(int P, int it) { const int x[7] = {0, 2, 5, 5, 6, 6, 7};
    return it >= 40 ? 99 : (P ? (it < 10 ? 2 * it : it - 20) : (it < 10 ? x[it] : (it < 20 ? it - 6 : (it < 24 ? it - 20 : it - 16)))); }
constexpr int g_bar(int P, int g) { return (P || g <= 13) ? (((g & 1) && g < 27) ? (g + 1) / 2 : -1) : ((g & 1) || g > 32 ? -1 : (g + 2) / 2); }
constexpr int g_item(int P, int g, int u) {
    const int t[23] = {22, 10, 2, 3, 11, 4, 5, 12, 6, 13, 24, 25, 26, 27, 28, -1, 29, -1, 30, 31, 32, -1, 33};
    const int t1[23] = {22, 2, 24, 25, 3, 26, 4, 27, 5, 28, 29, 6, 30, 31, -1, 32, -1, 33, -1, -1, -1, -1, -1};
    return u ? (g == 0 ? 23 : -1) : (g < 23 ? (P ? t1[g] : t[g]) : -1); }
constexpr int g_dd(int P, int H, int g) { return (H == 1 && !P) ? (g == 23 ? 0 : (g == 24 ? 1 : (g == 26 ? 2 : (g == 27 ? 3 : (g == 28 ? 4 : (g == 30 ? 5 : -1)))))) : -1; }
constexpr int g_st(int P, int g) { return (!P && g >= 16 && g < 19) ? g - 16 : -1; }
constexpr int ST_PUB = 14;
constexpr int vm_count(int P, int H, int gs) {
    int tag[96] = {}, cnt[96] = {}, m = 0; const int n = gs < 0 ? 0 : g_bar(P, gs);
    { const int t[4] = {0, 20, 21, 1}; for (int i = 0; i < 4; ++i) { tag[m] = t[i]; cnt[m++] = 2; } tag[m] = 97; cnt[m++] = NTOUCH; }
    for (int g = 0; g < gs; ++g) {
        for (int u = 0; u < 2; ++u) if (g_item(P, g, u) >= 0) { tag[m] = g_item(P, g, u); cnt[m++] = 2; }
        if (g_dd(P, H, g) >= 0) { tag[m] = 40; cnt[m++] = 4; }
        if (g_st(P, g) >= 0) { tag[m] = 98; cnt[m++] = 1; }
    }
    int last = -1;
    for (int i = 0; i < m; ++i) if (it_first(P, tag[i]) <= n || (tag[i] == 98 && n == ST_PUB)) last = i;
    int r = 0; for (int i = last + 1; i < m; ++i) r += cnt[i];
    return r;
}
struct P1 { lptr lds; unsigned l0, xsw; int lane; WSrc sx, sq, sf; OutC oc; unsigned xf[3], wf[3], xq[3], wq[3]; rsrc_t rws; u64 tag; u32x4 sd[3]; unsigned so[3], fqo, pollo; u32x2 fq; };
template <int P, int IT> DI void issue1(const P1& c, int p) {
    const WSrc& s = IT < 10 ? c.sx : (IT < 20 ? c.sq : c.sf);
    dma16(s.r, p ? s.v1 : s.v0, (IT < 10 ? IT : (IT < 20 ? IT - 10 : IT - 20)) * s.step, c.l0 + it_reg(P, IT) + p * 1024);
}
template <int IT> DI void issue2(const P1& c) { issue1<0, IT>(c, 0); issue1<0, IT>(c, 1); }
struct Frags { bf16x8 x[4], w[4]; };
DI lptr at(const P1& c, const unsigned (&b)[3], unsigned off) { return c.lds + b[off >> 16] + (off & 65535); }
template <int P, int G> DI void rd_part(Frags& f, const P1& c, int i) {
    if constexpr (P || G < 8 || G >= 15) {
        constexpr int K = (P || G < 8) ? G : G - 7;
        f.x[i] = lds_b128(at(c, c.xf, it_reg(P, K >> 2) + i * 4096) + (((2 * (K & 3)) ^ c.xsw) << 4));
        lptr wp = at(c, c.wf, it_reg(P, 20 + (K >> 1)) + (K & 1) * 2048 + i * 4096);
        f.w[i] = cat(lds_tr(wp), lds_tr(wp + 512));
    } else {
#pragma unroll
        for (int u = 2 * i; u < 2 * i + 2; ++u) {
            f.x[u] = lds_b128(at(c, c.xq, it_reg(0, G - 8)) + (((2 * u) ^ c.xsw) << 4));
            lptr wp = at(c, c.wq, it_reg(0, 10 + ((G - 8) >> 1)) + ((G & 1) * 4 + u) * 2048);
            f.w[u] = cat(lds_tr(wp), lds_tr(wp + 512));
        }
    }
}
template <int H, int G, int MODE> DI void group(P1& c, Frags& fc, Frags& fn, f32x16 (&acc)[4], f32x16& aq) {
    constexpr int P = MODE == 2; constexpr bool NEXT = G + 1 < (P ? 28 : 35), QG = !P && G >= 8 && G < 15;
    constexpr int I0 = g_item(P, G, 0), I1 = g_item(P, G, 1), DD = g_dd(P, H, G), ST = g_st(P, G);
    if constexpr (g_bar(P, G) >= 0) {
        waitv<vm_count(P, H, G)>(); BARRIER();
        if constexpr (H == 0 && g_bar(P, G) == ST_PUB) set_flag(c.rws, c.fqo, c.tag);
        if constexpr (H == 0 && G == 32) asm volatile("buffer_load_dwordx2 %0, %1, %2, 0 offen sc1" : "=v"(c.fq) : "v"(c.pollo), "s"(c.rws) : "memory");
    }
    if constexpr (H == 1 && G == 31) { if (c.lane < 32) *(LAS unsigned*)(c.lds + L_CNT + c.lane * 4) = 0; }
    SBAR();
#pragma unroll
    for (int i = 0; i < 4; ++i) {
        if constexpr (!QG) acc[i] = MFMA(fc.w[i & 1], fc.x[i >> 1], acc[i]);
        else if constexpr (MODE == 1) aq = MFMA(fc.x[i], fc.w[i], aq);
        else if constexpr (MODE == 0) aq = MFMA(fc.w[i], fc.x[i], aq);
        if constexpr (NEXT) { if (i < 2) rd_part<P, NEXT ? G + 1 : G>(fn, c, i); }
        SBAR();
        if constexpr (I1 >= 0) { if (i < 2) issue1<P, I0 >= 0 ? I0 : 0>(c, i); else issue1<P, I1 >= 0 ? I1 : 0>(c, i - 2); SBAR(); }
        else if constexpr (I0 >= 0) { if ((i >> 1) == H) { issue1<P, I0 >= 0 ? I0 : 0>(c, i & 1); SBAR(); } }
        if constexpr (DD >= 0) { out_issueW1(c.oc, DD >= 0 ? DD : 0, i); SBAR(); }
        if constexpr (ST >= 0) { if (i == 2 * H + 1) { st_sc1(c.sd[ST >= 0 ? ST : 0], c.rws, c.so[ST >= 0 ? ST : 0]); SBAR(); } }
    }
}
template <int H, int G0, int G1, int MODE> DI void groups(P1& c, Frags& fa, Frags& fb, f32x16 (&acc)[4], f32x16& aq) {
    if constexpr (G0 < G1) {
        if constexpr (G0 & 1) group<H, G0, MODE>(c, fb, fa, acc, aq); else group<H, G0, MODE>(c, fa, fb, acc, aq);
        groups<H, G0 + 1, G1, MODE>(c, fa, fb, acc, aq);
    }
}
DI void epi_qk(const f32x16& a, float sc, u32x4* sd) {
#pragma unroll
    for (int p = 0; p < 2; ++p) {
        unsigned a0 = cvtpk(a[8 * p] * sc, a[8 * p + 1] * sc), a1 = cvtpk(a[8 * p + 2] * sc, a[8 * p + 3] * sc), b0 = cvtpk(a[8 * p + 4] * sc, a[8 * p + 5] * sc), b1 = cvtpk(a[8 * p + 6] * sc, a[8 * p + 7] * sc);
        auto s0 = __builtin_amdgcn_permlane32_swap(a0, b0, false, false); auto s1 = __builtin_amdgcn_permlane32_swap(a1, b1, false, false);
        sd[p] = (u32x4){s0[0], s1[0], s0[1], s1[1]};
    }
}
DI void epi_h(const f32x16& g, const f32x16& u, u32x4* d) {
    f32x16 hv;
#pragma unroll
    for (int i = 0; i < 16; ++i) hv[i] = u[i] * sigmoidf_(g[i]);
    d[0] = pack8(hv, 0); d[1] = pack8(hv, 1);
}
DI void store_h(const u32x4 (&d)[4], unsigned o, rsrc_t rws) {
#pragma unroll
    for (int i = 0; i < 4; ++i) st_sc1(d[i], rws, o + (i >> 1) * 112 * 1024 + (i & 1) * 1024);
}
template <int H> DI void phase1(P1& c, f32x16 (&acc)[4], unsigned cg, unsigned rb, unsigned wid) {
    Frags fa, fb; f32x16 aq = {};
    const int lane = c.lane; const rsrc_t rws = c.rws;
#pragma unroll
    for (int i = 0; i < 4; ++i) acc[i] = (f32x16){};
    waitv<vm_count(0, H, -1)>(); BARRIER();
    const unsigned rp = wid & 1, pi = wid >> 1, tt = rb * 4 + (wid & 3);
    if (cg >= 14) {
        asm volatile("" ::: "memory");
        rd_part<0, 0>(fa, c, 0); rd_part<0, 0>(fa, c, 1);
        groups<H, 0, 28, 2>(c, fa, fb, acc, aq);
#pragma unroll
        for (int i = 0; i < 4; ++i) { u32x4 sd[2]; const unsigned kb = 4 * cg - 56 + H * 2 + (i & 1);
            const unsigned o = (kb < 7 ? WS_K : OOB) + kb * 4096 + (pi & 1) * 2048 + (rb * 4 + 2 * rp + (i >> 1)) * 28672 + lane * 16;
            epi_qk(acc[i], 1.f, sd); st_sc1(sd[0], rws, o); st_sc1(sd[1], rws, o + 1024); }
        if constexpr (H == 1) {
#pragma unroll
            for (int t = 0; t < 2; ++t) __builtin_amdgcn_raw_buffer_store_b64((u32x2){__float_as_uint(sigmoidf_(acc[2 * t + 1][0] + acc[2 * t + 1][5])), (unsigned)c.tag}, rws,
                                                                              (cg == 15 && pi == 2 && lane < 32) ? WS_G + ((rb * 4 + 2 * rp + t) * 32 + lane) * 8 : OOB, 0, 16);
        }
        if (H == 1) { if (lane < 32) *(LAS unsigned*)(c.lds + L_CNT + lane * 4) = 0; }
        waitv<0>();
        BARRIER();
        if (H == 0) { c.fq = (u32x2){0, 0}; set_flag(rws, (wid == 0 && lane < 4 && 4 * cg - 56 + lane < 7) ? WS_FQ + ((4 * cg - 49 + lane) * 16 + rb) * 8 : OOB, c.tag); }
        else {
#pragma unroll
            for (int i = 0; i < OB_NS; ++i) out_issueW(c.oc, i);
        }
        return;
    }
    asm volatile("" ::: "memory");
    rd_part<0, 0>(fa, c, 0); rd_part<0, 0>(fa, c, 1);
    groups<H, 0, 8, 0>(c, fa, fb, acc, aq);
    c.sd[2] = (u32x4){0x3f803f80u, 0x3f803f80u, 0x3f803f80u, 0x3f803f80u}; c.so[2] = OOB;
    if (cg < 7) {
        groups<H, 8, 16, 1>(c, fa, fb, acc, aq);
        const unsigned n = cg * 64 + H * 32 + (lane & 31), hd = n / 28, ph = (cg * 64 + 27) / 28 + (lane >> 2);
        c.sd[0] = pack8(aq, 0); c.sd[1] = pack8(aq, 1);
        c.so[0] = WS_V + (hd * 64 + tt) * 2048 + (n - 28 * hd + (lane & 32)) * 16;
        if (H == 0) c.so[2] = ph * 28 < (cg + 1) * 64 ? WS_V + (ph * 64 + tt) * 2048 + (lane & 1) * 1024 + (28 + 16 * (lane & 2)) * 16 : OOB;
    } else {
        groups<H, 8, 16, 0>(c, fa, fb, acc, aq);
        epi_qk(aq, QSCALE, c.sd);
        c.so[0] = WS_Q + (cg - 7) * 4096 + H * 2048 + tt * 28672 + lane * 16;
    }
    c.so[1] = c.so[0] + 1024;
    groups<H, 16, 35, 0>(c, fa, fb, acc, aq);
    if constexpr (H == 0) asm volatile("s_waitcnt vmcnt(0)" : "+v"(c.fq) :: "memory");
    BARRIER();
}

DI float max16(const f32x16& p) {
    float a = fmaxf(p[0], p[1]);
#pragma unroll
    for (int i = 2; i < 16; ++i) a = fmaxf(a, p[i]);
    auto rr = __builtin_amdgcn_permlane32_swap(__float_as_uint(a), __float_as_uint(a), false, false);
    return fmaxf(__uint_as_float(rr[0]), __uint_as_float(rr[1]));
}
#define EXP2(v) __builtin_amdgcn_exp2f(v)
DI void attn_part(char* ws, rsrc_t rws, lptr lds, unsigned wid, int lane, unsigned h, unsigned rb, u64 tag, unsigned ho, const f32x16 (&acc)[4], bool ready, unsigned pollo) {
    const unsigned hh = lane >> 5, va = lane * 16, vv = (lane & 31) > 28 ? OOB : va;
    const unsigned c0 = (7 * h >> 1) * 512;
    const rsrc_t rk = mk_rsrc(ws + WS_K + c0, 64 * 28672 - c0), rv = mk_rsrc(ws + WS_V + h * (64 * 2048), 64 * 2048);
    const unsigned kb = wid * (16 * 28672), vb = wid * (16 * 2048);
    bf16x8 q[4][2], kv[4][4];
    auto ldq = [&](unsigned t, int s) {
        u32x4 v = __builtin_bit_cast(u32x4, ld_sc1(rws, va, WS_Q + c0 + t * 28672 + s * 1024));
        const bool z = s ? (!(h & 1) && hh) : ((h & 1) && !hh);
        v[2 * s] = z ? 0u : v[2 * s]; v[2 * s + 1] = z ? 0u : v[2 * s + 1]; return __builtin_bit_cast(bf16x8, v); };
    auto ldkv = [&](bf16x8 (&d)[4], unsigned t) {
        d[0] = ld_sc1(rk, va, kb + t * 28672); d[1] = ld_sc1(rk, va, kb + t * 28672 + 1024); d[2] = ld_sc1(rv, vv, vb + t * 2048); d[3] = ld_sc1(rv, vv, vb + t * 2048 + 1024); };
    u32x4 qraw[4][2]; bf16x8 kz0, kz1;
    auto first_loads = [&]() {
#pragma unroll
        for (int i = 0; i < 4; ++i) { const unsigned qo = WS_Q + c0 + (rb * 4 + ((wid + 1 + i) & 3)) * 28672;
            qraw[i][0] = __builtin_bit_cast(u32x4, ld_sc1(rws, va, qo)); qraw[i][1] = __builtin_bit_cast(u32x4, ld_sc1(rws, va, qo + 1024)); }
        kz0 = ld_sc1(rk, va, 0); kz1 = ld_sc1(rk, va, 1024);
#pragma unroll
        for (int t = 0; t < 4; ++t) ldkv(kv[t], t);
    };
    u32x2 f2 = {0, 0};
    if (__builtin_expect(ready, 1)) first_loads(); else f2 = __builtin_amdgcn_raw_buffer_load_b64(rws, pollo, 0, 16);
    SBAR();
    u32x4 hd[4]; epi_h(acc[0], acc[1], hd); epi_h(acc[2], acc[3], hd + 2);
    PIN(hd[0]); PIN(hd[1]); PIN(hd[2]); PIN(hd[3]);
    if (__builtin_expect(!ready, 0)) { if (!__all((f2[0] == (unsigned)tag) & (f2[1] == (unsigned)(tag >> 32)))) wait_flags<1>(rws, pollo, tag); asm volatile("" ::: "memory"); first_loads(); }
    asm volatile("" ::: "memory");
    *(volatile LAS unsigned*)(lds + L_CNT + 32 + wid * 4) = 1u;
    store_h(hd, ho, rws);
    SBAR();
    const f32x16 zero = {};
    float mx = -INFINITY;
#pragma unroll
    for (int i = 0; i < 4; ++i) {
        const bool z0 = (h & 1) && !hh, z1 = !(h & 1) && hh;
        qraw[i][0][0] = z0 ? 0u : qraw[i][0][0]; qraw[i][0][1] = z0 ? 0u : qraw[i][0][1]; qraw[i][1][2] = z1 ? 0u : qraw[i][1][2]; qraw[i][1][3] = z1 ? 0u : qraw[i][1][3];
        q[i][0] = __builtin_bit_cast(bf16x8, qraw[i][0]); q[i][1] = __builtin_bit_cast(bf16x8, qraw[i][1]);
        f32x16 s = MFMA(kz0, q[i][0], zero); s = MFMA(kz1, q[i][1], s);
#pragma unroll
        for (int r = 0; r < 16; ++r) mx = fmaxf(mx, s[r]);
    }
    { auto rr = __builtin_amdgcn_permlane32_swap(__float_as_uint(mx), __float_as_uint(mx), false, false); mx = -fmaxf(__uint_as_float(rr[0]), __uint_as_float(rr[1])); }
    f32x16 cm;
#pragma unroll
    for (int r = 0; r < 16; ++r) cm[r] = mx;
    PIN(cm);
    f32x16 o[4], sA, sB; u32x4 p0, p1 = {0, 0, 0, 0};
#pragma unroll
    for (int i = 0; i < 4; ++i) o[i] = zero;
    sA = MFMA(kv[0][0], q[0][0], cm); sA = MFMA(kv[0][1], q[0][1], sA);
    u32x2 gv;
    float tp0 = 0.f, tp1 = 0.f, tp2 = 0.f, tp3 = 0.f;
    auto wr_o = [&](int i) { const unsigned own = (wid + 1 + i) & 3; lptr d = lds + COMB(own) + i * 4096 + va;
#pragma unroll
        for (int g = 0; g < 4; ++g) *(LAS f32x4*)(d + g * 1024) = (f32x4){o[i][4 * g], o[i][4 * g + 1], o[i][4 * g + 2], o[i][4 * g + 3]}; };
#define UNIT(SC, SN, I, KN0, KN1, IN, V0, VP1, IP, LOADS) do { \
    SN = MFMA(KN0, q[IN][0], cm); SBAR(); p1[2] = cvtpk(tp0, tp1); p1[3] = cvtpk(tp2, tp3); PIN(p1); SBAR(); SC[0] = EXP2(SC[0]); SC[1] = EXP2(SC[1]); SC[2] = EXP2(SC[2]); SC[3] = EXP2(SC[3]); PIN(SC); SBAR(); \
    SN = MFMA(KN1, q[IN][1], SN); SBAR(); SC[4] = EXP2(SC[4]); SC[5] = EXP2(SC[5]); p0[0] = cvtpk(SC[0], SC[1]); SC[6] = EXP2(SC[6]); SC[7] = EXP2(SC[7]); p0[1] = cvtpk(SC[2], SC[3]); PIN(SC); PIN(p0); SBAR(); \
    o[IP] = MFMA(VP1, __builtin_bit_cast(bf16x8, p1), o[IP]); SBAR(); p0[2] = cvtpk(SC[4], SC[5]); p0[3] = cvtpk(SC[6], SC[7]); PIN(p0); SBAR(); \
    SC[8] = EXP2(SC[8]); SC[9] = EXP2(SC[9]); SC[10] = EXP2(SC[10]); SC[11] = EXP2(SC[11]); PIN(SC); SBAR(); \
    LOADS; \
    o[I] = MFMA(V0, __builtin_bit_cast(bf16x8, p0), o[I]); SBAR(); tp0 = EXP2(SC[12]); tp1 = EXP2(SC[13]); p1[0] = cvtpk(SC[8], SC[9]); tp2 = EXP2(SC[14]); tp3 = EXP2(SC[15]); p1[1] = cvtpk(SC[10], SC[11]); \
    PIN(tp0); PIN(tp1); PIN(tp2); PIN(tp3); PIN(p1); SBAR(); } while (0)
#define TILE(T) do { \
    UNIT(sA, sB, 0, kv[(T) & 3][0], kv[(T) & 3][1], 1, kv[(T) & 3][2], kv[(T) ? ((T) + 3) & 3 : 0][3], 3, { if ((T) > 0 && (T) + 3 < 16) { ldkv(kv[((T) + 3) & 3], (T) + 3); SBAR(); } }); \
    UNIT(sB, sA, 1, kv[(T) & 3][0], kv[(T) & 3][1], 2, kv[(T) & 3][2], kv[(T) & 3][3], 0, {}); \
    UNIT(sA, sB, 2, kv[(T) & 3][0], kv[(T) & 3][1], 3, kv[(T) & 3][2], kv[(T) & 3][3], 1, { if ((T) == 15) { wr_o(0); SBAR(); } }); \
    UNIT(sB, sA, 3, kv[((T) + 1) & 3][0], kv[((T) + 1) & 3][1], 0, kv[(T) & 3][2], kv[(T) & 3][3], 2, { if ((T) == 15) { wr_o(1); SBAR(); } }); } while (0)
    TILE(0); TILE(1);
    waitv<4>(); *(volatile LAS unsigned*)(lds + L_CNT + 64 + wid * 4) = 1u; SBAR();
    TILE(2); TILE(3); TILE(4); TILE(5); TILE(6); TILE(7); TILE(8); TILE(9); TILE(10); TILE(11); TILE(12);
    gv = __builtin_amdgcn_raw_buffer_load_b64(rws, WS_G + (rb * 128 + wid * 32 + (lane & 31)) * 8, 0, 16); SBAR();
    TILE(13); TILE(14);
    *(volatile LAS unsigned*)(lds + L_CNT + 48 + wid * 4) = 1u;
    __builtin_amdgcn_s_setprio(2); SBAR();
    TILE(15);
#undef TILE
#undef UNIT
    p1[2] = cvtpk(tp0, tp1); p1[3] = cvtpk(tp2, tp3);
    o[3] = MFMA(kv[3][3], __builtin_bit_cast(bf16x8, p1), o[3]);
    wr_o(2);
    PIN(o[3]);
    group_barrier(lds + L_CNT, wid);
    f32x16 r = o[3];
#pragma unroll
    for (int j = 0; j < 3; ++j)
#pragma unroll
        for (int g = 0; g < 4; ++g) { const f32x4 v = *(LAS f32x4*)(lds + COMB(wid) + (j * 4 + g) * 1024 + va); r[4 * g] += v[0]; r[4 * g + 1] += v[1]; r[4 * g + 2] += v[2]; r[4 * g + 3] += v[3]; }
    const unsigned tt = rb * 4 + wid, oo = WS_O + (tt * 16 + h) * 2048 + va, fo = lane == 0 ? WS_FO + ((tt >> 1) * 32 + h * 2 + (tt & 1)) * 8 : OOB;
    auto finish = [&](f32x16 v, float gtok) {
        auto rr = __builtin_amdgcn_permlane32_swap(__float_as_uint(v[12]), __float_as_uint(v[12]), false, false);
        const float inv = gtok * __builtin_amdgcn_rcpf(__uint_as_float(rr[1]));
#pragma unroll
        for (int i = 0; i < 16; ++i) v[i] = (hh && i >= 12) ? 0.f : v[i] * inv;
        st_sc1(pack8(v, 0), rws, oo); st_sc1(pack8(v, 1), rws, oo + 1024);
        waitv<0>();
        set_flag(rws, fo, tag);
    };
    bool ok = true;
#pragma unroll
    for (int i = 0; i < 16; ++i) ok &= fabsf(r[i]) < 1.8e19f;
    ok &= !hh || r[12] > 5.4e-20f;
    if (__builtin_expect(__all(ok && gv[1] == (unsigned)tag), 1)) { finish(r, __uint_as_float(gv[0])); return; }
    if (!__all(ok)) {
        wait_flags<6>(rws, WS_FQ + lane * 8, tag, 21 * 128);
        const bf16x8 q0 = ldq(rb * 4 + wid, 0), q1 = ldq(rb * 4 + wid, 1);
        float m = -INFINITY; r = zero;
#pragma unroll 1
        for (int t = 0; t < 64; ++t) {
            const bf16x8 k0 = ld_sc1(rk, va, t * 28672), k1 = ld_sc1(rk, va, t * 28672 + 1024), v0 = ld_sc1(rv, vv, t * 2048), v1 = ld_sc1(rv, vv, t * 2048 + 1024);
            f32x16 s = MFMA(k0, q0, zero); s = MFMA(k1, q1, s);
            const float mn = fmaxf(m, max16(s)), al = EXP2(m - mn); m = mn;
#pragma unroll
            for (int i = 0; i < 16; ++i) { s[i] = EXP2(s[i] - mn); r[i] *= al; }
            r = MFMA(v0, __builtin_bit_cast(bf16x8, pack8(s, 0)), r); r = MFMA(v1, __builtin_bit_cast(bf16x8, pack8(s, 1)), r);
        }
    }
    asm volatile("; slow path" ::: "memory");
    finish(r, load_gate(rws, rb * 128 + wid * 32 + (lane & 31), tag));
}

DI void out_issueA(const OutC& c, int i, u32x4 (&a)[4]) {
#pragma unroll
    for (int t = 0; t < 2; ++t)
#pragma unroll
        for (int s = 0; s < 2; ++s)
            gload16_sc1(a[2 * t + s], c.rws, c.va, i < 14 ? WS_H + ((c.rb * 2 + (t ^ (c.w >> 1))) * 112 + c.w * 28 + 2 * i + s) * 1024 : WS_O + (((c.rb * 2 + (t ^ (c.w >> 1))) * 16 + c.w * 4 + (i - 14)) * 2 + s) * 1024);
}
template <int N> DI void waitv4(u32x4 (&a)[4]) { asm volatile("s_waitcnt vmcnt(%4)" : "+v"(a[0]), "+v"(a[1]), "+v"(a[2]), "+v"(a[3]) : "n"(N) : "memory"); }
constexpr int out_younger(int i) { int n = 0;
    for (int k = i + 1; k < OB_P; ++k) n += 4;
    for (int j = (i - OB_P + 1 > 0 ? i - OB_P + 1 : 0); j < i; ++j) { if (j + OB_NS < 18) n += 4; if (j + OB_P < 14) n += 4; }
    return n; }
template <int I> DI void out_stage(const OutC& c, lptr lds, const unsigned (&woff)[3][2], u32x4 (&af)[OB_P][4], f32x16 (&acc)[2][2]) {
    u32x4(&a)[4] = af[I % OB_P];
    waitv4<out_younger(I)>(a);
    lptr sl = lds + (RG(I % OB_NS) & 65535);
#pragma unroll
    for (int s = 0; s < 2; ++s) {
        bf16x8 wf[2];
#pragma unroll
        for (int cc = 0; cc < 2; ++cc) wf[cc] = cat(lds_tr(sl + woff[RG(I % OB_NS) >> 16][cc] + s * 2048), lds_tr(sl + woff[RG(I % OB_NS) >> 16][cc] + s * 2048 + 1024));
#pragma unroll
        for (int t = 0; t < 2; ++t)
#pragma unroll
            for (int cc = 0; cc < 2; ++cc) acc[t][cc] = MFMA(wf[cc], __builtin_bit_cast(bf16x8, a[2 * t + s]), acc[t][cc]);
    }
    SBAR();
    if constexpr (I + OB_NS < 18) out_issueW(c, I + OB_NS);
    if constexpr (I + OB_P < 14) out_issueA(c, I + OB_P, a);
}
template <int I0, int I1> DI void out_stages(const OutC& c, lptr lds, const unsigned (&woff)[3][2], u32x4 (&af)[OB_P][4], f32x16 (&acc)[2][2]) {
    if constexpr (I0 < I1) { out_stage<I0>(c, lds, woff, af, acc); out_stages<I0 + 1, I1>(c, lds, woff, af, acc); }
}
DI void out_part(const OutC& c, char* ws, uint16_t* out, lptr lds, int lane, unsigned nb, u64 tag) {
    const unsigned r32 = lane & 31, hh = lane >> 5, w = c.w, rb = c.rb;
    u32x4 af[OB_P][4];
    const u32x2 gv0 = __builtin_amdgcn_raw_buffer_load_b64(c.rws, WS_G + (rb * 64 + (w >> 1) * 32 + r32) * 8, 0, 16), gv1 = __builtin_amdgcn_raw_buffer_load_b64(c.rws, WS_G + (rb * 64 + ((w >> 1) ^ 1) * 32 + r32) * 8, 0, 16);
    if (w == 0) {
        __builtin_amdgcn_s_sleep(PSLEEP);
        wait_flags<1, 10>(c.rws, WS_FH + ((rb >> 1) * 16 + ((lane & 15) < 14 ? lane & 15 : 0)) * 8, tag);
        *(volatile LAS unsigned*)(lds + L_CNT + 96) = 1u;
    } else lds_wait(lds + L_CNT + 96, 1);
#pragma unroll
    for (int i = 0; i < OB_P; ++i) out_issueA(c, i, af[i]);
    f32x16 ab[2][2] = {};
    const unsigned q = (lane & 15) >> 2;
    unsigned woff[3][2];
#pragma unroll
    for (int cc = 0; cc < 2; ++cc)
#pragma unroll
        for (int u = 0; u < 3; ++u) { woff[u][cc] = u * 65536 + w * 4096 + (4 * hh + q) * 128 + ((cc ^ (w & 1) ^ (q >> 1)) << 6) + ((lane >> 4) & 1) * 32 + (lane & 3) * 8; PIN(woff[u][cc]); }
    __builtin_amdgcn_s_setprio(0);
    out_stages<0, PAUSE>(c, lds, woff, af, ab);
    if (PAUSE < 14) {
#pragma unroll 1
        for (int spin = 0; spin < 100000; ++spin) { if (rfl(*(volatile LAS unsigned*)(lds + L_CNT + 48 + w * 4))) break; __builtin_amdgcn_s_sleep(8); }
        asm volatile("" ::: "memory");
    }
    out_stages<PAUSE, 14>(c, lds, woff, af, ab);
    __builtin_amdgcn_s_setprio(1);
    {
        float g0 = __uint_as_float(gv0[0]), g1 = __uint_as_float(gv1[0]);
        if (__builtin_expect(!__all((gv0[1] == (unsigned)tag) & (gv1[1] == (unsigned)tag)), 0)) { g0 = load_gate(c.rws, rb * 64 + (w >> 1) * 32 + r32, tag); g1 = load_gate(c.rws, rb * 64 + ((w >> 1) ^ 1) * 32 + r32, tag); }
#pragma unroll
        for (int cc = 0; cc < 2; ++cc) { ab[0][cc] *= 1.f - g0; ab[1][cc] *= 1.f - g1; PIN(ab[0][cc]); PIN(ab[1][cc]); }
    }
    bf16x8 wo_f[4][2][2];
    waitv<0>();
#pragma unroll
    for (int i = 0; i < 4; ++i)
#pragma unroll
        for (int s = 0; s < 2; ++s)
#pragma unroll
            for (int cc = 0; cc < 2; ++cc) { lptr p = lds + (RG((14 + i) % OB_NS) & 65535) + woff[RG((14 + i) % OB_NS) >> 16][cc] + s * 2048; wo_f[i][s][cc] = cat(lds_tr(p), lds_tr(p + 1024)); PIN(wo_f[i][s][cc]); }
    wait_flags<1>(c.rws, WS_FO + (rb * 32 + w * 8 + (lane & 7)) * 8, tag);
#pragma unroll
    for (int i = 0; i < 4; ++i) out_issueA(c, 14 + i, af[i]);
#define OSTAGE(i) do { waitv4<(3 - (i)) * 4>(af[i]); \
    _Pragma("unroll") for (int s = 0; s < 2; ++s) _Pragma("unroll") for (int l = 3; l >= 0; --l) ab[l >> 1][l & 1] = MFMA(wo_f[i][s][l & 1], __builtin_bit_cast(bf16x8, af[i][2 * (l >> 1) + s]), ab[l >> 1][l & 1]); SBAR(); } while (0)
    OSTAGE(0); OSTAGE(1); OSTAGE(2); OSTAGE(3);
#undef OSTAGE
#pragma unroll
    for (int l = 3; l >= 1; --l)
#pragma unroll
        for (int g = 0; g < 4; ++g)
            *(LAS f32x4*)(lds + RG(l) + w * 4096 + g * 1024 + lane * 16) = (f32x4){ab[l >> 1][l & 1][4 * g], ab[l >> 1][l & 1][4 * g + 1], ab[l >> 1][l & 1][4 * g + 2], ab[l >> 1][l & 1][4 * g + 3]};
    PIN(ab[0][0]);
    group_barrier(lds + L_CNT + 16, w);
    f32x16 r = ab[0][0];
#pragma unroll
    for (int l = 1; l < 4; ++l)
#pragma unroll
        for (int g = 0; g < 4; ++g) { const f32x4 v = *(LAS f32x4*)(lds + RG(l) + (w ^ l) * 4096 + g * 1024 + lane * 16); r[4 * g] += v[0]; r[4 * g + 1] += v[1]; r[4 * g + 2] += v[2]; r[4 * g + 3] += v[3]; }
    u32x4 sd[2]; epi_qk(r, 1.f, sd);
    const rsrc_t ro = mk_rsrc(out, SEQ * DM * 2); const unsigned oo = ((rb * 64 + (w >> 1) * 32 + r32) * DM + nb * 64 + (w & 1) * 32 + 8 * hh) * 2;
    __builtin_amdgcn_raw_buffer_store_b128(sd[0], ro, oo, 0, 0); __builtin_amdgcn_raw_buffer_store_b128(sd[1], ro, oo + 32, 0, 0);
}

__global__ __launch_bounds__(512, 2) void fused(const uint16_t* x, const uint16_t* wqkv, const uint16_t* wo, const uint16_t* wg, const uint16_t* wu,
                                                const uint16_t* wd, const uint16_t* gw, char* ws, uint16_t* out) {
    extern __shared__ __attribute__((aligned(16))) char smem[];
    lptr lds = (lptr)smem;
    u64 pc; asm volatile("s_getpc_b64 %0" : "=s"(pc));
    const int tid = threadIdx.x, lane = tid & 63; const unsigned wid = rfl(tid >> 6);
    const unsigned b = blockIdx.x, xc = b & 7, j = b >> 3;
    u64 tag;
    const rsrc_t rws = mk_rsrc(ws, WS_END);
    OutC oc; oc.rd = mk_rsrc(wd, FF * DM * 2); oc.ro = mk_rsrc(wo, DM * DM * 2); oc.rws = rws; oc.w = wid & 3; oc.rb = xc * 4 + (j & 3);
    oc.l0 = (unsigned)(uintptr_t)lds + oc.w * 4096; oc.va = lane * 16;
    oc.vw = (lane >> 3) * (DM * 2) + (j < 28 ? (j >> 2) * 128 : OOB) + (((lane & 7) ^ (((lane >> 4) & 1) << 2)) << 4);
    const unsigned rb = 8 * (xc & 1) + (j & 7), cg = 4 * (j >> 3) + (xc >> 1);
    const unsigned rp = wid & 1, pi = wid >> 1, rt = wid & 3, half = wid >> 2;
    const unsigned h = 2 * xc + (j & 1), ab = j >> 1;
    P1 c; c.lds = lds; c.l0 = (unsigned)(uintptr_t)lds + wid * 2048; c.lane = lane; c.oc = oc; c.rws = rws;
    {
        const unsigned xr = 16 * wid + (lane >> 3), qb = cg < 7 ? 14 + cg : cg - 7;
        c.sx.r = mk_rsrc(x, SEQ * DM * 2); c.sx.step = 128;
        c.sx.v0 = (rb * 128 + xr) * (DM * 2) + (((lane & 7) ^ ((xr >> 1) & 7)) << 4); c.sx.v1 = (rb * 128 + xr + 8) * (DM * 2) + (((lane & 7) ^ (((xr + 8) >> 1) & 7)) << 4);
        issue2<0>(c);
        const bool ff = cg < 14; const unsigned pitch = ff ? FF * 2 : 3 * DM * 2, a0 = 2 * cg + half, a1 = 4 * cg - 49 + pi, fcol = (ff ? a0 : a1) * 128;
        c.sf = mk_wsrc(mk_rsrc(ff ? ((wid & 2) ? wu : wg) : wqkv, DM * pitch), pitch, fcol, 16 * rp, 32, lane);
        if (__builtin_expect(cg == 15 && pi == 3, 0)) {
            const unsigned sc = (lane & 7) ^ (((lane >> 4) & 1) << 2), r = 16 * rp + (lane >> 3);
            c.sf.r = mk_rsrc(gw, DM * 2); c.sf.step = 64; c.sf.v0 = sc == (r & 1) ? (r & ~1u) * 2 : OOB; c.sf.v1 = c.sf.v0 + 16;
        }
        issue2<20>(c);
        { u64 pe = (u64)(ws + WS_E); asm volatile("" : "+s"(pe)); tag = *(const __attribute__((address_space(4))) u64*)pe; }
        __builtin_amdgcn_s_barrier();
        issue2<21>(c); issue2<1>(c);
        {
            const unsigned to = (128 + ((j & 7) * 2 + rp) * 20 + lane) * pitch + fcol;
            dma4(c.sf.r, lane < 20 ? to : OOB, c.l0 + it_reg(0, 22));
            dma4(mk_rsrc((const void*)pc, CODE_PF), lane < 2 ? (lane * 256 + j * 8 + wid) * 128 : OOB, c.l0 + it_reg(0, 22));
        }
        c.sq = mk_wsrc(mk_rsrc(wqkv, ff ? DM * 3 * DM * 2 : 0), 3 * DM * 2, qb * 128, 16 * wid, 128, lane);
        const unsigned r32 = lane & 31, hh = lane >> 5, q = (lane & 15) >> 2, wl = (8 * hh + q) * 128 + ((lane >> 4) & 1) * 32 + (lane & 3) * 8;
        c.xsw = hh ^ ((r32 >> 1) & 7);
#pragma unroll
        for (int u = 0; u < 3; ++u) {
            c.xf[u] = u * 65536 + (64 * rp + r32) * 128; c.xq[u] = u * 65536 + (32 * rt + r32) * 128;
            c.wf[u] = u * 65536 + half * 8192 + wl + (((pi & 1) ^ (q >> 1)) << 6); c.wq[u] = u * 65536 + wl + ((half ^ (q >> 1)) << 6);
            PIN(c.xf[u]); PIN(c.xq[u]); PIN(c.wf[u]); PIN(c.wq[u]);
        }
    }
    asm volatile("" : "+s"(tag), "+s"(out));
    tag += 0x9E3779B97F4A7C15ull; tag = (tag ^ (tag >> 30)) * 0xBF58476D1CE4E5B9ull; tag = (tag ^ (tag >> 27)) * 0x94D049BB133111EBull; tag ^= tag >> 31;
    c.tag = tag;
    f32x16 acc[4];
    const unsigned fh = cg < 14 ? WS_FH + (rb * 16 + cg) * 8 : OOB, ho = (cg < 14 ? WS_H : OOB) + ((rb * 4 + 2 * rp) * 112 + (cg * 4 + pi) * 2) * 1024 + lane * 16;
    if (wid < 4) {
        const unsigned hb = (28 * h + ((lane & 4) ? 27 : 0)) >> 6, grp = (lane >> 3) & 3;
        c.pollo = WS_FQ + ((grp == 3 ? hb : (grp == 1 ? 14 + hb : 7 + hb)) * 16 + (grp < 2 ? rt * 4 + (lane & 3) : (grp == 2 ? 0 : ab))) * 8;
        c.fqo = (tid == 0 && cg < 14) ? WS_FQ + ((cg < 7 ? 14 + cg : cg - 7) * 16 + rb) * 8 : OOB;
        phase1<0>(c, acc, cg, rb, wid);
        attn_part(ws, rws, lds, wid, lane, h, ab, tag, ho, acc, __all((c.fq[0] == (unsigned)tag) & (c.fq[1] == (unsigned)(tag >> 32))), c.pollo);
        __builtin_amdgcn_endpgm();
    }
    {
        phase1<1>(c, acc, cg, rb, wid);
        if (cg < 14) {
            u32x4 hd[4]; epi_h(acc[0], acc[1], hd); epi_h(acc[2], acc[3], hd + 2);
            PIN(hd[0]); PIN(hd[1]); PIN(hd[2]); PIN(hd[3]);
            lds_wait(lds + L_CNT + 32 + rt * 4, 1);
            store_h(hd, ho, rws);
        }
        __builtin_amdgcn_s_setprio(3);
        waitv<0>(); *(volatile LAS unsigned*)(lds + L_CNT + 64 + wid * 4) = 1u;
        if (wid == 4) {
#pragma unroll 1
            for (int spin = 0; spin < 4000000; ++spin) {
                const u32x4 v0 = *(volatile LAS u32x4*)(lds + L_CNT + 64), v1 = *(volatile LAS u32x4*)(lds + L_CNT + 80);
                if (rfl(v0[0] & v0[1] & v0[2] & v0[3] & v1[0] & v1[1] & v1[2] & v1[3])) break;
                __builtin_amdgcn_s_sleep(2);
            }
            asm volatile("" ::: "memory");
            set_flag(rws, lane == 0 ? fh : OOB, tag);
        }
        if (j < 28) out_part(oc, ws, out, lds, lane, j >> 2, tag);
        else if (b == 255 && wid == 4) {
            wait_flags<6, 20>(rws, WS_FQ + lane * 8, tag, 21 * 128);
            set_flag(rws, lane == 0 ? WS_E : OOB, tag);
        }
        __builtin_amdgcn_endpgm();
    }
}

extern "C" void kernel_launch(void* const* d_in, const int* in_sizes, int n_in, void* d_out, int out_size, void* d_ws, size_t ws_size, hipStream_t stream) {
    (void)hipFuncSetAttribute((const void*)fused, hipFuncAttributeMaxDynamicSharedMemorySize, LDS_BYTES);
    fused<<<256, 512, LDS_BYTES, stream>>>((const uint16_t*)d_in[0], (const uint16_t*)d_in[1], (const uint16_t*)d_in[2], (const uint16_t*)d_in[4], (const uint16_t*)d_in[5],
                                           (const uint16_t*)d_in[6], (const uint16_t*)d_in[7], (char*)d_ws, (uint16_t*)d_out);
}
```

```cpp
#include <hip/hip_runtime.h>
#include <hip/hip_bf16.h>
#include <stdint.h>

typedef short bf16x8 __attribute__((ext_vector_type(8)));
typedef short s16x4 __attribute__((ext_vector_type(4)));
typedef float f32x16 __attribute__((ext_vector_type(16)));
typedef float f32x4 __attribute__((ext_vector_type(4)));
typedef float f32x2 __attribute__((ext_vector_type(2)));
typedef unsigned u32x4 __attribute__((ext_vector_type(4)));
typedef unsigned u32x2 __attribute__((ext_vector_type(2)));
typedef __bf16 bf16x2_t __attribute__((ext_vector_type(2)));
typedef unsigned long long u64;
typedef __amdgpu_buffer_rsrc_t rsrc_t;
#define LAS __attribute__((address_space(3)))
#define DI __device__ __forceinline__
typedef LAS char* lptr;

constexpr int SEQ = 2048, DM = 448, DH = 28, FF = 1792;
constexpr float LOG2E = 1.4426950408889634f;
constexpr float QSCALE = 0.18898223650461362f * LOG2E;

constexpr unsigned WS_Q = 0;
constexpr unsigned WS_K = WS_Q + (2u << 20);
constexpr unsigned WS_V = WS_K + (2u << 20);
constexpr unsigned WS_O = WS_V + (2u << 20);
constexpr unsigned WS_H = WS_O + (2u << 20);
constexpr unsigned WS_G = WS_H + 64u * 112 * 1024;
constexpr unsigned WS_FQ = WS_G + 16384;
constexpr unsigned WS_FH = WS_FQ + 8192;
constexpr unsigned WS_FO = WS_FH + 16384;
constexpr unsigned WS_E = WS_FO + 8192;
constexpr unsigned WS_END = WS_E + 256;
constexpr unsigned CODE_PF = 50000;
constexpr int PSLEEP = 10, PAUSE = 7, NTOUCH = 1;
constexpr unsigned OOB = 0x7ffffff0u;

constexpr int LDS_BYTES = 160 * 1024;
constexpr unsigned RG(int i) { const int t[6] = {4, 0, 1, 3, 7, 2}; return t[i] * 16384; }
#define COMB(w) (((0x8596u >> (4 * (w))) & 15u) << 14)
constexpr unsigned L_CNT = 6 * 16384 + 12288;

#define MFMA(a, b, c) __builtin_amdgcn_mfma_f32_32x32x16_bf16((a), (b), (c), 0, 0, 0)
#define SBAR() __builtin_amdgcn_sched_barrier(0)
#define PIN(x) asm volatile("" : "+v"(x))
#define BARRIER() asm volatile("s_waitcnt lgkmcnt(0)\n\ts_barrier" ::: "memory")
template <int N> DI void waitv() { asm volatile("s_waitcnt vmcnt(%0)" :: "n"(N) : "memory"); }


DI rsrc_t mk_rsrc(const void* p, unsigned bytes) { return __builtin_amdgcn_make_buffer_rsrc((void*)p, (short)0, (int)bytes, 0x00020000); }
DI unsigned rfl(unsigned v) { return (unsigned)__builtin_amdgcn_readfirstlane((int)v); }
DI void dma16(rsrc_t r, unsigned voff, unsigned soff, unsigned ldsaddr) {
    asm volatile("s_mov_b32 m0, %0\n\ts_nop 0\n\tbuffer_load_dwordx4 %1, %2, %3 offen lds" :: "s"(ldsaddr), "v"(voff), "s"(r), "s"(soff) : "memory");
}
DI void dma4(rsrc_t r, unsigned voff, unsigned ldsaddr) {
    asm volatile("s_mov_b32 m0, %0\n\ts_nop 0\n\tbuffer_load_dword %1, %2, 0 offen lds" :: "s"(ldsaddr), "v"(voff), "s"(r) : "memory");
}
DI void gload16_sc1(u32x4& dst, rsrc_t r, unsigned voff, unsigned soff) {
    asm volatile("buffer_load_dwordx4 %0, %1, %2, %3 offen sc1" : "=v"(dst) : "v"(voff), "s"(r), "s"(soff) : "memory");
}
DI bf16x8 ld_sc1(rsrc_t r, unsigned voff, unsigned soff) { return __builtin_bit_cast(bf16x8, __builtin_amdgcn_raw_buffer_load_b128(r, voff, soff, 16)); }
DI void st_sc1(u32x4 v, rsrc_t r, unsigned voff) { __builtin_amdgcn_raw_buffer_store_b128(v, r, voff, 0, 16); }
DI unsigned cvtpk(float lo, float hi) { return __builtin_bit_cast(unsigned, __builtin_convertvector((f32x2){lo, hi}, bf16x2_t)); }
DI bf16x8 lds_b128(lptr p) { return *(const LAS bf16x8*)p; }
DI s16x4 lds_tr(lptr p) { return __builtin_amdgcn_ds_read_tr16_b64_v4i16((LAS s16x4*)p); }
DI bf16x8 cat(s16x4 lo, s16x4 hi) { return __builtin_shufflevector(lo, hi, 0, 1, 2, 3, 4, 5, 6, 7); }
DI u32x4 pack8(const f32x16& a, int s) {
    u32x4 r; r[0] = cvtpk(a[8 * s], a[8 * s + 1]); r[1] = cvtpk(a[8 * s + 2], a[8 * s + 3]); r[2] = cvtpk(a[8 * s + 4], a[8 * s + 5]); r[3] = cvtpk(a[8 * s + 6], a[8 * s + 7]); return r;
}
DI float sigmoidf_(float v) { return __builtin_amdgcn_rcpf(1.f + __builtin_amdgcn_exp2f(-LOG2E * v)); }

DI void set_flag(rsrc_t rws, unsigned off, u64 tag) {
    __builtin_amdgcn_raw_buffer_store_b64((u32x2){(unsigned)tag, (unsigned)(tag >> 32)}, rws, off, 0, 16);
}
template <int NW, int SLEEP = 4> DI void wait_flags(rsrc_t rws, unsigned off, u64 tag, unsigned lim = ~0u) {
#pragma unroll 1
    for (int spin = 0; spin < 40000; ++spin) {
        bool ok = true;
        asm volatile("" ::: "memory");
#pragma unroll
        for (int i = 0; i < NW; ++i) { const u32x2 v = __builtin_amdgcn_raw_buffer_load_b64(rws, off + i * 512, 0, 16); ok &= ((v[0] == (unsigned)tag) & (v[1] == (unsigned)(tag >> 32))) | (off + i * 512 - WS_FQ >= lim); }
        if (__all(ok)) break;
        __builtin_amdgcn_s_sleep(SLEEP);
    }
    asm volatile("" ::: "memory");
}
DI float load_gate(rsrc_t rws, unsigned tok, u64 tag) {
    u32x2 v;
#pragma unroll 1
    for (int spin = 0; spin < 40000; ++spin) {
        asm volatile("" ::: "memory");
        v = __builtin_amdgcn_raw_buffer_load_b64(rws, WS_G + tok * 8, 0, 16);
        if (__all(v[1] == (unsigned)tag)) break;
        __builtin_amdgcn_s_sleep(2);
    }
    return __uint_as_float(v[0]);
}
DI void lds_wait(lptr cnt, unsigned target) {
#pragma unroll 1
    for (int spin = 0; spin < 4000000; ++spin) {
        if (rfl(__hip_atomic_load((LAS unsigned*)cnt, __ATOMIC_RELAXED, __HIP_MEMORY_SCOPE_WORKGROUP)) >= target) break;
        __builtin_amdgcn_s_sleep(4);
    }
    asm volatile("" ::: "memory");
}
DI void group_barrier(lptr words, unsigned w) {
    asm volatile("s_waitcnt lgkmcnt(0)" ::: "memory");
    *(volatile LAS unsigned*)(words + w * 4) = 1u;
#pragma unroll 1
    for (int spin = 0; spin < 4000000; ++spin) {
        const u32x4 v = *(volatile LAS u32x4*)words;
        if (rfl(v[0] & v[1] & v[2] & v[3])) break;
    }
    asm volatile("" ::: "memory");
}

struct WSrc { rsrc_t r; unsigned v0, v1, step; };
DI WSrc mk_wsrc(rsrc_t r, unsigned pitch, unsigned colb, unsigned row0, unsigned rows_per_stage, int lane) {
    WSrc s; s.r = r; const unsigned sw = ((lane & 7) ^ (((lane >> 4) & 1) << 2)) << 4;
    s.v0 = (row0 + (lane >> 3)) * pitch + colb + sw; s.v1 = s.v0 + 8 * pitch; s.step = rows_per_stage * pitch; return s;
}
constexpr int OB_NS = 6, OB_P = 4;
struct OutC { rsrc_t rd, ro, rws; unsigned l0, vw, va, w, rb; };
DI void out_issueW1(const OutC& c, int i, int p) {
    const unsigned so = i < 14 ? (c.w * 448 + i * 32) * (DM * 2) : (c.w * 4 + (i - 14)) * DH * (DM * 2);
    dma16(i < 14 ? c.rd : c.ro, c.vw, so + p * 8 * (DM * 2), c.l0 + RG(i % OB_NS) + p * 1024);
}
DI void out_issueW(const OutC& c, int i) {
#pragma unroll
    for (int p = 0; p < 4; ++p) out_issueW1(c, i, p);
}
constexpr unsigned it_reg(int P, int it) { const int x[7] = {0, 3, 7, 8, 1, 2, 5}, q[4] = {6, 9, 4, 0}, f[14] = {1, 2, 4, 5, 3, 6, 9, 4, 0, 3, 7, 6, 9, 8};
    const int x1[7] = {0, 3, 6, 9, 2, 0, 3}, f1[14] = {1, 2, 4, 5, 7, 8, 1, 4, 5, 7, 8, 1, 4, 5};
    return (P ? (it < 10 ? x1[it] : f1[it - 20]) : (it < 10 ? x[it] : (it < 20 ? q[it - 10] : f[it - 20]))) * 16384; }
constexpr int it_first(int P, int it) { const int x[7] = {0, 2, 5, 5, 6, 6, 7};
    return it >= 40 ? 99 : (P ? (it < 10 ? 2 * it : it - 20) : (it < 10 ? x[it] : (it < 20 ? it - 6 : (it < 24 ? it - 20 : it - 16)))); }
constexpr int g_bar(int P, int g) { return (P || g <= 13) ? (((g & 1) && g < 27) ? (g + 1) / 2 : -1) : ((g & 1) || g > 32 ? -1 : (g + 2) / 2); }
constexpr int g_item(int P, int g, int u) {
    const int t[23] = {22, 10, 2, 3, 11, 4, 5, 12, 6, 13, 24, 25, 26, 27, 28, -1, 29, -1, 30, 31, 32, -1, 33};
    const int t1[23] = {22, 2, 24, 25, 3, 26, 4, 27, 5, 28, 29, 6, 30, 31, -1, 32, -1, 33, -1, -1, -1, -1, -1};
    return u ? (g == 0 ? 23 : -1) : (g < 23 ? (P ? t1[g] : t[g]) : -1); }
constexpr int g_dd(int P, int H, int g) { return (H == 1 && !P) ? (g == 23 ? 0 : (g == 24 ? 1 : (g == 26 ? 2 : (g == 27 ? 3 : (g == 28 ? 4 : (g == 30 ? 5 : -1)))))) : -1; }
constexpr int g_st(int P, int g) { return (!P && g >= 16 && g < 19) ? g - 16 : -1; }
constexpr int ST_PUB = 14;
constexpr int vm_count(int P, int H, int gs) {
    int tag[96] = {}, cnt[96] = {}, m = 0; const int n = gs < 0 ? 0 : g_bar(P, gs);
    { const int t[4] = {0, 20, 21, 1}; for (int i = 0; i < 4; ++i) { tag[m] = t[i]; cnt[m++] = 2; } tag[m] = 97; cnt[m++] = NTOUCH; }
    for (int g = 0; g < gs; ++g) {
        for (int u = 0; u < 2; ++u) if (g_item(P, g, u) >= 0) { tag[m] = g_item(P, g, u); cnt[m++] = 2; }
        if (g_dd(P, H, g) >= 0) { tag[m] = 40; cnt[m++] = 4; }
        if (g_st(P, g) >= 0) { tag[m] = 98; cnt[m++] = 1; }
    }
    int last = -1;
    for (int i = 0; i < m; ++i) if (it_first(P, tag[i]) <= n || (tag[i] == 98 && n == ST_PUB)) last = i;
    int r = 0; for (int i = last + 1; i < m; ++i) r += cnt[i];
    return r;
}
struct P1 { lptr lds; unsigned l0, xsw; int lane; WSrc sx, sq, sf; OutC oc; unsigned xf[3], wf[3], xq[3], wq[3]; rsrc_t rws; u64 tag; u32x4 sd[3]; unsigned so[3], fqo, pollo; u32x2 fq; };
template <int P, int IT> DI void issue1(const P1& c, int p) {
    const WSrc& s = IT < 10 ? c.sx : (IT < 20 ? c.sq : c.sf);
    dma16(s.r, p ? s.v1 : s.v0, (IT < 10 ? IT : (IT < 20 ? IT - 10 : IT - 20)) * s.step, c.l0 + it_reg(P, IT) + p * 1024);
}
template <int IT> DI void issue2(const P1& c) { issue1<0, IT>(c, 0); issue1<0, IT>(c, 1); }
struct Frags { bf16x8 x[4], w[4]; };
DI lptr at(const P1& c, const unsigned (&b)[3], unsigned off) { return c.lds + b[off >> 16] + (off & 65535); }
template <int P, int G> DI void rd_part(Frags& f, const P1& c, int i) {
    if constexpr (P || G < 8 || G >= 15) {
        constexpr int K = (P || G < 8) ? G : G - 7;
        f.x[i] = lds_b128(at(c, c.xf, it_reg(P, K >> 2) + i * 4096) + (((2 * (K & 3)) ^ c.xsw) << 4));
        lptr wp = at(c, c.wf, it_reg(P, 20 + (K >> 1)) + (K & 1) * 2048 + i * 4096);
        f.w[i] = cat(lds_tr(wp), lds_tr(wp + 512));
    } else {
#pragma unroll
        for (int u = 2 * i; u < 2 * i + 2; ++u) {
            f.x[u] = lds_b128(at(c, c.xq, it_reg(0, G - 8)) + (((2 * u) ^ c.xsw) << 4));
            lptr wp = at(c, c.wq, it_reg(0, 10 + ((G - 8) >> 1)) + ((G & 1) * 4 + u) * 2048);
            f.w[u] = cat(lds_tr(wp), lds_tr(wp + 512));
        }
    }
}
template <int H, int G, int MODE> DI void group(P1& c, Frags& fc, Frags& fn, f32x16 (&acc)[4], f32x16& aq) {
    constexpr int P = MODE == 2; constexpr bool NEXT = G + 1 < (P ? 28 : 35), QG = !P && G >= 8 && G < 15;
    constexpr int I0 = g_item(P, G, 0), I1 = g_item(P, G, 1), DD = g_dd(P, H, G), ST = g_st(P, G);
    if constexpr (g_bar(P, G) >= 0) {
        waitv<vm_count(P, H, G)>(); BARRIER();
        if constexpr (H == 0 && g_bar(P, G) == ST_PUB) set_flag(c.rws, c.fqo, c.tag);
        if constexpr (H == 0 && G == 32) asm volatile("buffer_load_dwordx2 %0, %1, %2, 0 offen sc1" : "=v"(c.fq) : "v"(c.pollo), "s"(c.rws) : "memory");
    }
    if constexpr (H == 1 && G == 31) { if (c.lane < 32) *(LAS unsigned*)(c.lds + L_CNT + c.lane * 4) = 0; }
    SBAR();
#pragma unroll
    for (int i = 0; i < 4; ++i) {
        if constexpr (!QG) acc[i] = MFMA(fc.w[i & 1], fc.x[i >> 1], acc[i]);
        else if constexpr (MODE == 1) aq = MFMA(fc.x[i], fc.w[i], aq);
        else if constexpr (MODE == 0) aq = MFMA(fc.w[i], fc.x[i], aq);
        if constexpr (NEXT) { if (i < 2) rd_part<P, NEXT ? G + 1 : G>(fn, c, i); }
        SBAR();
        if constexpr (I1 >= 0) { if (i < 2) issue1<P, I0 >= 0 ? I0 : 0>(c, i); else issue1<P, I1 >= 0 ? I1 : 0>(c, i - 2); SBAR(); }
        else if constexpr (I0 >= 0) { if ((i >> 1) == H) { issue1<P, I0 >= 0 ? I0 : 0>(c, i & 1); SBAR(); } }
        if constexpr (DD >= 0) { out_issueW1(c.oc, DD >= 0 ? DD : 0, i); SBAR(); }
        if constexpr (ST >= 0) { if (i == 2 * H + 1) { st_sc1(c.sd[ST >= 0 ? ST : 0], c.rws, c.so[ST >= 0 ? ST : 0]); SBAR(); } }
    }
}
template <int H, int G0, int G1, int MODE> DI void groups(P1& c, Frags& fa, Frags& fb, f32x16 (&acc)[4], f32x16& aq) {
    if constexpr (G0 < G1) {
        if constexpr (G0 & 1) group<H, G0, MODE>(c, fb, fa, acc, aq); else group<H, G0, MODE>(c, fa, fb, acc, aq);
        groups<H, G0 + 1, G1, MODE>(c, fa, fb, acc, aq);
    }
}
DI void epi_qk(const f32x16& a, float sc, u32x4* sd) {
#pragma unroll
    for (int p = 0; p < 2; ++p) {
        unsigned a0 = cvtpk(a[8 * p] * sc, a[8 * p + 1] * sc), a1 = cvtpk(a[8 * p + 2] * sc, a[8 * p + 3] * sc), b0 = cvtpk(a[8 * p + 4] * sc, a[8 * p + 5] * sc), b1 = cvtpk(a[8 * p + 6] * sc, a[8 * p + 7] * sc);
        auto s0 = __builtin_amdgcn_permlane32_swap(a0, b0, false, false); auto s1 = __builtin_amdgcn_permlane32_swap(a1, b1, false, false);
        sd[p] = (u32x4){s0[0], s1[0], s0[1], s1[1]};
    }
}
DI void epi_h(const f32x16& g, const f32x16& u, u32x4* d) {
    f32x16 hv;
#pragma unroll
    for (int i = 0; i < 16; ++i) hv[i] = u[i] * sigmoidf_(g[i]);
    d[0] = pack8(hv, 0); d[1] = pack8(hv, 1);
}
DI void store_h(const u32x4 (&d)[4], unsigned o, rsrc_t rws) {
#pragma unroll
    for (int i = 0; i < 4; ++i) st_sc1(d[i], rws, o + (i >> 1) * 112 * 1024 + (i & 1) * 1024);
}
template <int H> DI void phase1(P1& c, f32x16 (&acc)[4], unsigned cg, unsigned rb, unsigned wid) {
    Frags fa, fb; f32x16 aq = {};
    const int lane = c.lane; const rsrc_t rws = c.rws;
#pragma unroll
    for (int i = 0; i < 4; ++i) acc[i] = (f32x16){};
    waitv<vm_count(0, H, -1)>(); BARRIER();
    const unsigned rp = wid & 1, pi = wid >> 1, tt = rb * 4 + (wid & 3);
    if (cg >= 14) {
        asm volatile("" ::: "memory");
        rd_part<0, 0>(fa, c, 0); rd_part<0, 0>(fa, c, 1);
        groups<H, 0, 28, 2>(c, fa, fb, acc, aq);
#pragma unroll
        for (int i = 0; i < 4; ++i) { u32x4 sd[2]; const unsigned kb = 4 * cg - 56 + H * 2 + (i & 1);
            const unsigned o = (kb < 7 ? WS_K : OOB) + kb * 4096 + (pi & 1) * 2048 + (rb * 4 + 2 * rp + (i >> 1)) * 28672 + lane * 16;
            epi_qk(acc[i], 1.f, sd); st_sc1(sd[0], rws, o); st_sc1(sd[1], rws, o + 1024); }
        if constexpr (H == 1) {
#pragma unroll
            for (int t = 0; t < 2; ++t) __builtin_amdgcn_raw_buffer_store_b64((u32x2){__float_as_uint(sigmoidf_(acc[2 * t + 1][0] + acc[2 * t + 1][5])), (unsigned)c.tag}, rws,
                                                                              (cg == 15 && pi == 2 && lane < 32) ? WS_G + ((rb * 4 + 2 * rp + t) * 32 + lane) * 8 : OOB, 0, 16);
        }
        if (H == 1) { if (lane < 32) *(LAS unsigned*)(c.lds + L_CNT + lane * 4) = 0; }
        waitv<0>();
        BARRIER();
        if (H == 0) { c.fq = (u32x2){0, 0}; set_flag(rws, (wid == 0 && lane < 4 && 4 * cg - 56 + lane < 7) ? WS_FQ + ((4 * cg - 49 + lane) * 16 + rb) * 8 : OOB, c.tag); }
        else {
#pragma unroll
            for (int i = 0; i < OB_NS; ++i) out_issueW(c.oc, i);
        }
        return;
    }
    asm volatile("" ::: "memory");
    rd_part<0, 0>(fa, c, 0); rd_part<0, 0>(fa, c, 1);
    groups<H, 0, 8, 0>(c, fa, fb, acc, aq);
    c.sd[2] = (u32x4){0x3f803f80u, 0x3f803f80u, 0x3f803f80u, 0x3f803f80u}; c.so[2] = OOB;
    if (cg < 7) {
        groups<H, 8, 16, 1>(c, fa, fb, acc, aq);
        const unsigned n = cg * 64 + H * 32 + (lane & 31), hd = n / 28, ph = (cg * 64 + 27) / 28 + (lane >> 2);
        c.sd[0] = pack8(aq, 0); c.sd[1] = pack8(aq, 1);
        c.so[0] = WS_V + (hd * 64 + tt) * 2048 + (n - 28 * hd + (lane & 32)) * 16;
        if (H == 0) c.so[2] = ph * 28 < (cg + 1) * 64 ? WS_V + (ph * 64 + tt) * 2048 + (lane & 1) * 1024 + (28 + 16 * (lane & 2)) * 16 : OOB;
    } else {
        groups<H, 8, 16, 0>(c, fa, fb, acc, aq);
        epi_qk(aq, QSCALE, c.sd);
        c.so[0] = WS_Q + (cg - 7) * 4096 + H * 2048 + tt * 28672 + lane * 16;
    }
    c.so[1] = c.so[0] + 1024;
    groups<H, 16, 35, 0>(c, fa, fb, acc, aq);
    if constexpr (H == 0) asm volatile("s_waitcnt vmcnt(0)" : "+v"(c.fq) :: "memory");
    BARRIER();
}

DI float max16(const f32x16& p) {
    float a = fmaxf(p[0], p[1]);
#pragma unroll
    for (int i = 2; i < 16; ++i) a = fmaxf(a, p[i]);
    auto rr = __builtin_amdgcn_permlane32_swap(__float_as_uint(a), __float_as_uint(a), false, false);
    return fmaxf(__uint_as_float(rr[0]), __uint_as_float(rr[1]));
}
#define EXP2(v) __builtin_amdgcn_exp2f(v)
DI void attn_part(char* ws, rsrc_t rws, lptr lds, unsigned wid, int lane, unsigned h, unsigned rb, u64 tag, unsigned ho, const f32x16 (&acc)[4], bool ready, unsigned pollo) {
    const unsigned hh = lane >> 5, va = lane * 16, vv = (lane & 31) > 28 ? OOB : va;
    const unsigned c0 = (7 * h >> 1) * 512;
    const rsrc_t rk = mk_rsrc(ws + WS_K + c0, 64 * 28672 - c0), rv = mk_rsrc(ws + WS_V + h * (64 * 2048), 64 * 2048);
    const unsigned kb = wid * (16 * 28672), vb = wid * (16 * 2048);
    bf16x8 q[4][2], kv[4][4];
    auto ldq = [&](unsigned t, int s) {
        u32x4 v = __builtin_bit_cast(u32x4, ld_sc1(rws, va, WS_Q + c0 + t * 28672 + s * 1024));
        const bool z = s ? (!(h & 1) && hh) : ((h & 1) && !hh);
        v[2 * s] = z ? 0u : v[2 * s]; v[2 * s + 1] = z ? 0u : v[2 * s + 1]; return __builtin_bit_cast(bf16x8, v); };
    auto ldkv = [&](bf16x8 (&d)[4], unsigned t) {
        d[0] = ld_sc1(rk, va, kb + t * 28672); d[1] = ld_sc1(rk, va, kb + t * 28672 + 1024); d[2] = ld_sc1(rv, vv, vb + t * 2048); d[3] = ld_sc1(rv, vv, vb + t * 2048 + 1024); };
    u32x4 qraw[4][2]; bf16x8 kz0, kz1;
    auto first_loads = [&]() {
#pragma unroll
        for (int i = 0; i < 4; ++i) { const unsigned qo = WS_Q + c0 + (rb * 4 + ((wid + 1 + i) & 3)) * 28672;
            qraw[i][0] = __builtin_bit_cast(u32x4, ld_sc1(rws, va, qo)); qraw[i][1] = __builtin_bit_cast(u32x4, ld_sc1(rws, va, qo + 1024)); }
        kz0 = ld_sc1(rk, va, 0); kz1 = ld_sc1(rk, va, 1024);
#pragma unroll
        for (int t = 0; t < 4; ++t) ldkv(kv[t], t);
    };
    u32x2 f2 = {0, 0};
    if (__builtin_expect(ready, 1)) first_loads(); else f2 = __builtin_amdgcn_raw_buffer_load_b64(rws, pollo, 0, 16);
    SBAR();
    u32x4 hd[4]; epi_h(acc[0], acc[1], hd); epi_h(acc[2], acc[3], hd + 2);
    PIN(hd[0]); PIN(hd[1]); PIN(hd[2]); PIN(hd[3]);
    if (__builtin_expect(!ready, 0)) { if (!__all((f2[0] == (unsigned)tag) & (f2[1] == (unsigned)(tag >> 32)))) wait_flags<1>(rws, pollo, tag); asm volatile("" ::: "memory"); first_loads(); }
    asm volatile("" ::: "memory");
    *(volatile LAS unsigned*)(lds + L_CNT + 32 + wid * 4) = 1u;
    store_h(hd, ho, rws);
    SBAR();
    const f32x16 zero = {};
    float mx = -INFINITY;
#pragma unroll
    for (int i = 0; i < 4; ++i) {
        const bool z0 = (h & 1) && !hh, z1 = !(h & 1) && hh;
        qraw[i][0][0] = z0 ? 0u : qraw[i][0][0]; qraw[i][0][1] = z0 ? 0u : qraw[i][0][1]; qraw[i][1][2] = z1 ? 0u : qraw[i][1][2]; qraw[i][1][3] = z1 ? 0u : qraw[i][1][3];
        q[i][0] = __builtin_bit_cast(bf16x8, qraw[i][0]); q[i][1] = __builtin_bit_cast(bf16x8, qraw[i][1]);
        f32x16 s = MFMA(kz0, q[i][0], zero); s = MFMA(kz1, q[i][1], s);
#pragma unroll
        for (int r = 0; r < 16; ++r) mx = fmaxf(mx, s[r]);
    }
    { auto rr = __builtin_amdgcn_permlane32_swap(__float_as_uint(mx), __float_as_uint(mx), false, false); mx = -fmaxf(__uint_as_float(rr[0]), __uint_as_float(rr[1])); }
    f32x16 cm;
#pragma unroll
    for (int r = 0; r < 16; ++r) cm[r] = mx;
    PIN(cm);
    f32x16 o[4], sA, sB; u32x4 p0, p1 = {0, 0, 0, 0};
#pragma unroll
    for (int i = 0; i < 4; ++i) o[i] = zero;
    sA = MFMA(kv[0][0], q[0][0], cm); sA = MFMA(kv[0][1], q[0][1], sA);
    u32x2 gv;
    float tp0 = 0.f, tp1 = 0.f, tp2 = 0.f, tp3 = 0.f;
    auto wr_o = [&](int i) { const unsigned own = (wid + 1 + i) & 3; lptr d = lds + COMB(own) + i * 4096 + va;
#pragma unroll
        for (int g = 0; g < 4; ++g) *(LAS f32x4*)(d + g * 1024) = (f32x4){o[i][4 * g], o[i][4 * g + 1], o[i][4 * g + 2], o[i][4 * g + 3]}; };
#define UNIT(SC, SN, I, KN0, KN1, IN, V0, VP1, IP, LOADS) do { \
    SN = MFMA(KN0, q[IN][0], cm); SBAR(); p1[2] = cvtpk(tp0, tp1); p1[3] = cvtpk(tp2, tp3); PIN(p1); SBAR(); SC[0] = EXP2(SC[0]); SC[1] = EXP2(SC[1]); SC[2] = EXP2(SC[2]); SC[3] = EXP2(SC[3]); PIN(SC); SBAR(); \
    SN = MFMA(KN1, q[IN][1], SN); SBAR(); SC[4] = EXP2(SC[4]); SC[5] = EXP2(SC[5]); p0[0] = cvtpk(SC[0], SC[1]); SC[6] = EXP2(SC[6]); SC[7] = EXP2(SC[7]); p0[1] = cvtpk(SC[2], SC[3]); PIN(SC); PIN(p0); SBAR(); \
    o[IP] = MFMA(VP1, __builtin_bit_cast(bf16x8, p1), o[IP]); SBAR(); p0[2] = cvtpk(SC[4], SC[5]); p0[3] = cvtpk(SC[6], SC[7]); PIN(p0); SBAR(); \
    SC[8] = EXP2(SC[8]); SC[9] = EXP2(SC[9]); SC[10] = EXP2(SC[10]); SC[11] = EXP2(SC[11]); PIN(SC); SBAR(); \
    LOADS; \
    o[I] = MFMA(V0, __builtin_bit_cast(bf16x8, p0), o[I]); SBAR(); tp0 = EXP2(SC[12]); tp1 = EXP2(SC[13]); p1[0] = cvtpk(SC[8], SC[9]); tp2 = EXP2(SC[14]); tp3 = EXP2(SC[15]); p1[1] = cvtpk(SC[10], SC[11]); \
    PIN(tp0); PIN(tp1); PIN(tp2); PIN(tp3); PIN(p1); SBAR(); } while (0)
#define TILE(T) do { \
    UNIT(sA, sB, 0, kv[(T) & 3][0], kv[(T) & 3][1], 1, kv[(T) & 3][2], kv[(T) ? ((T) + 3) & 3 : 0][3], 3, { if ((T) > 0 && (T) + 3 < 16) { ldkv(kv[((T) + 3) & 3], (T) + 3); SBAR(); } }); \
    UNIT(sB, sA, 1, kv[(T) & 3][0], kv[(T) & 3][1], 2, kv[(T) & 3][2], kv[(T) & 3][3], 0, {}); \
    UNIT(sA, sB, 2, kv[(T) & 3][0], kv[(T) & 3][1], 3, kv[(T) & 3][2], kv[(T) & 3][3], 1, { if ((T) == 15) { wr_o(0); SBAR(); } }); \
    UNIT(sB, sA, 3, kv[((T) + 1) & 3][0], kv[((T) + 1) & 3][1], 0, kv[(T) & 3][2], kv[(T) & 3][3], 2, { if ((T) == 15) { wr_o(1); SBAR(); } }); } while (0)
    TILE(0); TILE(1);
    waitv<4>(); *(volatile LAS unsigned*)(lds + L_CNT + 64 + wid * 4) = 1u; SBAR();
    TILE(2); TILE(3); TILE(4); TILE(5); TILE(6); TILE(7); TILE(8); TILE(9); TILE(10); TILE(11); TILE(12);
    gv = __builtin_amdgcn_raw_buffer_load_b64(rws, WS_G + (rb * 128 + wid * 32 + (lane & 31)) * 8, 0, 16); SBAR();
    TILE(13); TILE(14);
    *(volatile LAS unsigned*)(lds + L_CNT + 48 + wid * 4) = 1u;
    __builtin_amdgcn_s_setprio(2); SBAR();
    TILE(15);
#undef TILE
#undef UNIT
    p1[2] = cvtpk(tp0, tp1); p1[3] = cvtpk(tp2, tp3);
    o[3] = MFMA(kv[3][3], __builtin_bit_cast(bf16x8, p1), o[3]);
    wr_o(2);
    PIN(o[3]);
    group_barrier(lds + L_CNT, wid);
    f32x16 r = o[3];
#pragma unroll
    for (int j = 0; j < 3; ++j)
#pragma unroll
        for (int g = 0; g < 4; ++g) { const f32x4 v = *(LAS f32x4*)(lds + COMB(wid) + (j * 4 + g) * 1024 + va); r[4 * g] += v[0]; r[4 * g + 1] += v[1]; r[4 * g + 2] += v[2]; r[4 * g + 3] += v[3]; }
    const unsigned tt = rb * 4 + wid, oo = WS_O + (tt * 16 + h) * 2048 + va, fo = lane == 0 ? WS_FO + ((tt >> 1) * 32 + h * 2 + (tt & 1)) * 8 : OOB;
    auto finish = [&](f32x16 v, float gtok) {
        auto rr = __builtin_amdgcn_permlane32_swap(__float_as_uint(v[12]), __float_as_uint(v[12]), false, false);
        const float inv = gtok * __builtin_amdgcn_rcpf(__uint_as_float(rr[1]));
#pragma unroll
        for (int i = 0; i < 16; ++i) v[i] = (hh && i >= 12) ? 0.f : v[i] * inv;
        st_sc1(pack8(v, 0), rws, oo); st_sc1(pack8(v, 1), rws, oo + 1024);
        waitv<0>();
        set_flag(rws, fo, tag);
    };
    bool ok = true;
#pragma unroll
    for (int i = 0; i < 16; ++i) ok &= fabsf(r[i]) < 1.8e19f;
    ok &= !hh || r[12] > 5.4e-20f;
    if (__builtin_expect(__all(ok && gv[1] == (unsigned)tag), 1)) { finish(r, __uint_as_float(gv[0])); return; }
    if (!__all(ok)) {
        wait_flags<6>(rws, WS_FQ + lane * 8, tag, 21 * 128);
        const bf16x8 q0 = ldq(rb * 4 + wid, 0), q1 = ldq(rb * 4 + wid, 1);
        float m = -INFINITY; r = zero;
#pragma unroll 1
        for (int t = 0; t < 64; ++t) {
            const bf16x8 k0 = ld_sc1(rk, va, t * 28672), k1 = ld_sc1(rk, va, t * 28672 + 1024), v0 = ld_sc1(rv, vv, t * 2048), v1 = ld_sc1(rv, vv, t * 2048 + 1024);
            f32x16 s = MFMA(k0, q0, zero); s = MFMA(k1, q1, s);
            const float mn = fmaxf(m, max16(s)), al = EXP2(m - mn); m = mn;
#pragma unroll
            for (int i = 0; i < 16; ++i) { s[i] = EXP2(s[i] - mn); r[i] *= al; }
            r = MFMA(v0, __builtin_bit_cast(bf16x8, pack8(s, 0)), r); r = MFMA(v1, __builtin_bit_cast(bf16x8, pack8(s, 1)), r);
        }
    }
    asm volatile("; slow path" ::: "memory");
    finish(r, load_gate(rws, rb * 128 + wid * 32 + (lane & 31), tag));
}

DI void out_issueA(const OutC& c, int i, u32x4 (&a)[4]) {
#pragma unroll
    for (int t = 0; t < 2; ++t)
#pragma unroll
        for (int s = 0; s < 2; ++s)
            gload16_sc1(a[2 * t + s], c.rws, c.va, i < 14 ? WS_H + ((c.rb * 2 + (t ^ (c.w >> 1))) * 112 + c.w * 28 + 2 * i + s) * 1024 : WS_O + (((c.rb * 2 + (t ^ (c.w >> 1))) * 16 + c.w * 4 + (i - 14)) * 2 + s) * 1024);
}
template <int N> DI void waitv4(u32x4 (&a)[4]) { asm volatile("s_waitcnt vmcnt(%4)" : "+v"(a[0]), "+v"(a[1]), "+v"(a[2]), "+v"(a[3]) : "n"(N) : "memory"); }
constexpr int out_younger(int i) { int n = 0;
    for (int k = i + 1; k < OB_P; ++k) n += 4;
    for (int j = (i - OB_P + 1 > 0 ? i - OB_P + 1 : 0); j < i; ++j) { if (j + OB_NS < 18) n += 4; if (j + OB_P < 14) n += 4; }
    return n; }
template <int I> DI void out_stage(const OutC& c, lptr lds, const unsigned (&woff)[3][2], u32x4 (&af)[OB_P][4], f32x16 (&acc)[2][2]) {
    u32x4(&a)[4] = af[I % OB_P];
    waitv4<out_younger(I)>(a);
    lptr sl = lds + (RG(I % OB_NS) & 65535);
#pragma unroll
    for (int s = 0; s < 2; ++s) {
        bf16x8 wf[2];
#pragma unroll
        for (int cc = 0; cc < 2; ++cc) wf[cc] = cat(lds_tr(sl + woff[RG(I % OB_NS) >> 16][cc] + s * 2048), lds_tr(sl + woff[RG(I % OB_NS) >> 16][cc] + s * 2048 + 1024));
#pragma unroll
        for (int t = 0; t < 2; ++t)
#pragma unroll
            for (int cc = 0; cc < 2; ++cc) acc[t][cc] = MFMA(wf[cc], __builtin_bit_cast(bf16x8, a[2 * t + s]), acc[t][cc]);
    }
    SBAR();
    if constexpr (I + OB_NS < 18) out_issueW(c, I + OB_NS);
    if constexpr (I + OB_P < 14) out_issueA(c, I + OB_P, a);
}
template <int I0, int I1> DI void out_stages(const OutC& c, lptr lds, const unsigned (&woff)[3][2], u32x4 (&af)[OB_P][4], f32x16 (&acc)[2][2]) {
    if constexpr (I0 < I1) { out_stage<I0>(c, lds, woff, af, acc); out_stages<I0 + 1, I1>(c, lds, woff, af, acc); }
}
DI void out_part(const OutC& c, char* ws, uint16_t* out, lptr lds, int lane, unsigned nb, u64 tag) {
    const unsigned r32 = lane & 31, hh = lane >> 5, w = c.w, rb = c.rb;
    u32x4 af[OB_P][4];
    const u32x2 gv0 = __builtin_amdgcn_raw_buffer_load_b64(c.rws, WS_G + (rb * 64 + (w >> 1) * 32 + r32) * 8, 0, 16), gv1 = __builtin_amdgcn_raw_buffer_load_b64(c.rws, WS_G + (rb * 64 + ((w >> 1) ^ 1) * 32 + r32) * 8, 0, 16);
    if (w == 0) {
        __builtin_amdgcn_s_sleep(PSLEEP);
        wait_flags<1, 10>(c.rws, WS_FH + ((rb >> 1) * 16 + ((lane & 15) < 14 ? lane & 15 : 0)) * 8, tag);
        *(volatile LAS unsigned*)(lds + L_CNT + 96) = 1u;
    } else lds_wait(lds + L_CNT + 96, 1);
#pragma unroll
    for (int i = 0; i < OB_P; ++i) out_issueA(c, i, af[i]);
    f32x16 ab[2][2] = {};
    const unsigned q = (lane & 15) >> 2;
    unsigned woff[3][2];
#pragma unroll
    for (int cc = 0; cc < 2; ++cc)
#pragma unroll
        for (int u = 0; u < 3; ++u) { woff[u][cc] = u * 65536 + w * 4096 + (4 * hh + q) * 128 + ((cc ^ (w & 1) ^ (q >> 1)) << 6) + ((lane >> 4) & 1) * 32 + (lane & 3) * 8; PIN(woff[u][cc]); }
    __builtin_amdgcn_s_setprio(0);
    out_stages<0, PAUSE>(c, lds, woff, af, ab);
    if (PAUSE < 14) {
#pragma unroll 1
        for (int spin = 0; spin < 100000; ++spin) { if (rfl(*(volatile LAS unsigned*)(lds + L_CNT + 48 + w * 4))) break; __builtin_amdgcn_s_sleep(8); }
        asm volatile("" ::: "memory");
    }
    out_stages<PAUSE, 14>(c, lds, woff, af, ab);
    __builtin_amdgcn_s_setprio(1);
    {
        float g0 = __uint_as_float(gv0[0]), g1 = __uint_as_float(gv1[0]);
        if (__builtin_expect(!__all((gv0[1] == (unsigned)tag) & (gv1[1] == (unsigned)tag)), 0)) { g0 = load_gate(c.rws, rb * 64 + (w >> 1) * 32 + r32, tag); g1 = load_gate(c.rws, rb * 64 + ((w >> 1) ^ 1) * 32 + r32, tag); }
#pragma unroll
        for (int cc = 0; cc < 2; ++cc) { ab[0][cc] *= 1.f - g0; ab[1][cc] *= 1.f - g1; PIN(ab[0][cc]); PIN(ab[1][cc]); }
    }
    bf16x8 wo_f[4][2][2];
    waitv<0>();
#pragma unroll
    for (int i = 0; i < 4; ++i)
#pragma unroll
        for (int s = 0; s < 2; ++s)
#pragma unroll
            for (int cc = 0; cc < 2; ++cc) { lptr p = lds + (RG((14 + i) % OB_NS) & 65535) + woff[RG((14 + i) % OB_NS) >> 16][cc] + s * 2048; wo_f[i][s][cc] = cat(lds_tr(p), lds_tr(p + 1024)); PIN(wo_f[i][s][cc]); }
    wait_flags<1>(c.rws, WS_FO + (rb * 32 + w * 8 + (lane & 7)) * 8, tag);
#pragma unroll
    for (int i = 0; i < 4; ++i) out_issueA(c, 14 + i, af[i]);
#define OSTAGE(i) do { waitv4<(3 - (i)) * 4>(af[i]); \
    _Pragma("unroll") for (int s = 0; s < 2; ++s) _Pragma("unroll") for (int l = 3; l >= 0; --l) ab[l >> 1][l & 1] = MFMA(wo_f[i][s][l & 1], __builtin_bit_cast(bf16x8, af[i][2 * (l >> 1) + s]), ab[l >> 1][l & 1]); SBAR(); } while (0)
    OSTAGE(0); OSTAGE(1); OSTAGE(2); OSTAGE(3);
#undef OSTAGE
#pragma unroll
    for (int l = 3; l >= 1; --l)
#pragma unroll
        for (int g = 0; g < 4; ++g)
            *(LAS f32x4*)(lds + RG(l) + w * 4096 + g * 1024 + lane * 16) = (f32x4){ab[l >> 1][l & 1][4 * g], ab[l >> 1][l & 1][4 * g + 1], ab[l >> 1][l & 1][4 * g + 2], ab[l >> 1][l & 1][4 * g + 3]};
    PIN(ab[0][0]);
    group_barrier(lds + L_CNT + 16, w);
    f32x16 r = ab[0][0];
#pragma unroll
    for (int l = 1; l < 4; ++l)
#pragma unroll
        for (int g = 0; g < 4; ++g) { const f32x4 v = *(LAS f32x4*)(lds + RG(l) + (w ^ l) * 4096 + g * 1024 + lane * 16); r[4 * g] += v[0]; r[4 * g + 1] += v[1]; r[4 * g + 2] += v[2]; r[4 * g + 3] += v[3]; }
    u32x4 sd[2]; epi_qk(r, 1.f, sd);
    const rsrc_t ro = mk_rsrc(out, SEQ * DM * 2); const unsigned oo = ((rb * 64 + (w >> 1) * 32 + r32) * DM + nb * 64 + (w & 1) * 32 + 8 * hh) * 2;
    __builtin_amdgcn_raw_buffer_store_b128(sd[0], ro, oo, 0, 0); __builtin_amdgcn_raw_buffer_store_b128(sd[1], ro, oo + 32, 0, 0);
}

__global__ __launch_bounds__(512, 2) void fused(const uint16_t* x, const uint16_t* wqkv, const uint16_t* wo, const uint16_t* wg, const uint16_t* wu,
                                                const uint16_t* wd, const uint16_t* gw, char* ws, uint16_t* out) {
    extern __shared__ __attribute__((aligned(16))) char smem[];
    lptr lds = (lptr)smem;
    u64 pc; asm volatile("s_getpc_b64 %0" : "=s"(pc));
    const int tid = threadIdx.x, lane = tid & 63; const unsigned wid = rfl(tid >> 6);
    const unsigned b = blockIdx.x, xc = b & 7, j = b >> 3;
    dma4(mk_rsrc((const void*)pc, CODE_PF), lane < 2 ? (lane * 256 + j * 8 + wid) * 128 : OOB, (unsigned)(uintptr_t)lds + wid * 2048 + it_reg(0, 22));
    SBAR();
    u64 tag;
    const rsrc_t rws = mk_rsrc(ws, WS_END);
    OutC oc; oc.rd = mk_rsrc(wd, FF * DM * 2); oc.ro = mk_rsrc(wo, DM * DM * 2); oc.rws = rws; oc.w = wid & 3; oc.rb = xc * 4 + (j & 3);
    oc.l0 = (unsigned)(uintptr_t)lds + oc.w * 4096; oc.va = lane * 16;
    oc.vw = (lane >> 3) * (DM * 2) + (j < 28 ? (j >> 2) * 128 : OOB) + (((lane & 7) ^ (((lane >> 4) & 1) << 2)) << 4);
    const unsigned rb = 8 * (xc & 1) + (j & 7), cg = 4 * (j >> 3) + (xc >> 1);
    const unsigned rp = wid & 1, pi = wid >> 1, rt = wid & 3, half = wid >> 2;
    const unsigned h = 2 * xc + (j & 1), ab = j >> 1;
    P1 c; c.lds = lds; c.l0 = (unsigned)(uintptr_t)lds + wid * 2048; c.lane = lane; c.oc = oc; c.rws = rws;
    {
        const unsigned xr = 16 * wid + (lane >> 3), qb = cg < 7 ? 14 + cg : cg - 7;
        c.sx.r = mk_rsrc(x, SEQ * DM * 2); c.sx.step = 128;
        c.sx.v0 = (rb * 128 + xr) * (DM * 2) + (((lane & 7) ^ ((xr >> 1) & 7)) << 4); c.sx.v1 = (rb * 128 + xr + 8) * (DM * 2) + (((lane & 7) ^ (((xr + 8) >> 1) & 7)) << 4);
        issue2<0>(c);
        const bool ff = cg < 14; const unsigned pitch = ff ? FF * 2 : 3 * DM * 2, a0 = 2 * cg + half, a1 = 4 * cg - 49 + pi, fcol = (ff ? a0 : a1) * 128;
        c.sf = mk_wsrc(mk_rsrc(ff ? ((wid & 2) ? wu : wg) : wqkv, DM * pitch), pitch, fcol, 16 * rp, 32, lane);
        if (__builtin_expect(cg == 15 && pi == 3, 0)) {
            const unsigned sc = (lane & 7) ^ (((lane >> 4) & 1) << 2), r = 16 * rp + (lane >> 3);
            c.sf.r = mk_rsrc(gw, DM * 2); c.sf.step = 64; c.sf.v0 = sc == (r & 1) ? (r & ~1u) * 2 : OOB; c.sf.v1 = c.sf.v0 + 16;
        }
        issue2<20>(c);
        { u64 pe = (u64)(ws + WS_E); asm volatile("" : "+s"(pe)); tag = *(const __attribute__((address_space(4))) u64*)pe; }
        __builtin_amdgcn_s_barrier();
        issue2<21>(c); issue2<1>(c);
        {
            const unsigned to = (64 + ((j & 7) * 2 + rp) + lane * 16) * pitch + fcol;
            dma4(c.sf.r, lane < 24 ? to : OOB, c.l0 + it_reg(0, 22));
        }
        c.sq = mk_wsrc(mk_rsrc(wqkv, ff ? DM * 3 * DM * 2 : 0), 3 * DM * 2, qb * 128, 16 * wid, 128, lane);
        const unsigned r32 = lane & 31, hh = lane >> 5, q = (lane & 15) >> 2, wl = (8 * hh + q) * 128 + ((lane >> 4) & 1) * 32 + (lane & 3) * 8;
        c.xsw = hh ^ ((r32 >> 1) & 7);
#pragma unroll
        for (int u = 0; u < 3; ++u) {
            c.xf[u] = u * 65536 + (64 * rp + r32) * 128; c.xq[u] = u * 65536 + (32 * rt + r32) * 128;
            c.wf[u] = u * 65536 + half * 8192 + wl + (((pi & 1) ^ (q >> 1)) << 6); c.wq[u] = u * 65536 + wl + ((half ^ (q >> 1)) << 6);
            PIN(c.xf[u]); PIN(c.xq[u]); PIN(c.wf[u]); PIN(c.wq[u]);
        }
    }
    asm volatile("" : "+s"(tag), "+s"(out));
    tag += 0x9E3779B97F4A7C15ull; tag = (tag ^ (tag >> 30)) * 0xBF58476D1CE4E5B9ull; tag = (tag ^ (tag >> 27)) * 0x94D049BB133111EBull; tag ^= tag >> 31;
    c.tag = tag;
    f32x16 acc[4];
    const unsigned fh = cg < 14 ? WS_FH + (rb * 16 + cg) * 8 : OOB, ho = (cg < 14 ? WS_H : OOB) + ((rb * 4 + 2 * rp) * 112 + (cg * 4 + pi) * 2) * 1024 + lane * 16;
    if (wid < 4) {
        const unsigned hb = (28 * h + ((lane & 4) ? 27 : 0)) >> 6, grp = (lane >> 3) & 3;
        c.pollo = WS_FQ + ((grp == 3 ? hb : (grp == 1 ? 14 + hb : 7 + hb)) * 16 + (grp < 2 ? rt * 4 + (lane & 3) : (grp == 2 ? 0 : ab))) * 8;
        c.fqo = (tid == 0 && cg < 14) ? WS_FQ + ((cg < 7 ? 14 + cg : cg - 7) * 16 + rb) * 8 : OOB;
        phase1<0>(c, acc, cg, rb, wid);
        attn_part(ws, rws, lds, wid, lane, h, ab, tag, ho, acc, __all((c.fq[0] == (unsigned)tag) & (c.fq[1] == (unsigned)(tag >> 32))), c.pollo);
        __builtin_amdgcn_endpgm();
    }
    {
        phase1<1>(c, acc, cg, rb, wid);
        if (cg < 14) {
            u32x4 hd[4]; epi_h(acc[0], acc[1], hd); epi_h(acc[2], acc[3], hd + 2);
            PIN(hd[0]); PIN(hd[1]); PIN(hd[2]); PIN(hd[3]);
            lds_wait(lds + L_CNT + 32 + rt * 4, 1);
            store_h(hd, ho, rws);
        }
        __builtin_amdgcn_s_setprio(3);
        waitv<0>(); *(volatile LAS unsigned*)(lds + L_CNT + 64 + wid * 4) = 1u;
        if (wid == 4) {
#pragma unroll 1
            for (int spin = 0; spin < 4000000; ++spin) {
                const u32x4 v0 = *(volatile LAS u32x4*)(lds + L_CNT + 64), v1 = *(volatile LAS u32x4*)(lds + L_CNT + 80);
                if (rfl(v0[0] & v0[1] & v0[2] & v0[3] & v1[0] & v1[1] & v1[2] & v1[3])) break;
                __builtin_amdgcn_s_sleep(2);
            }
            asm volatile("" ::: "memory");
            set_flag(rws, lane == 0 ? fh : OOB, tag);
        }
        if (j < 28) out_part(oc, ws, out, lds, lane, j >> 2, tag);
        else if (b == 255 && wid == 4) {
            wait_flags<6, 20>(rws, WS_FQ + lane * 8, tag, 21 * 128);
            set_flag(rws, lane == 0 ? WS_E : OOB, tag);
        }
        __builtin_amdgcn_endpgm();
    }
}

extern "C" void kernel_launch(void* const* d_in, const int* in_sizes, int n_in, void* d_out, int out_size, void* d_ws, size_t ws_size, hipStream_t stream) {
    (void)hipFuncSetAttribute((const void*)fused, hipFuncAttributeMaxDynamicSharedMemorySize, LDS_BYTES);
    fused<<<256, 512, LDS_BYTES, stream>>>((const uint16_t*)d_in[0], (const uint16_t*)d_in[1], (const uint16_t*)d_in[2], (const uint16_t*)d_in[4], (const uint16_t*)d_in[5],
                                           (const uint16_t*)d_in[6], (const uint16_t*)d_in[7], (char*)d_ws, (uint16_t*)d_out);
}
```

```cpp
#include <hip/hip_runtime.h>
#include <hip/hip_bf16.h>
#include <stdint.h>

typedef short bf16x8 __attribute__((ext_vector_type(8)));
typedef short s16x4 __attribute__((ext_vector_type(4)));
typedef float f32x16 __attribute__((ext_vector_type(16)));
typedef float f32x4 __attribute__((ext_vector_type(4)));
typedef float f32x2 __attribute__((ext_vector_type(2)));
typedef unsigned u32x4 __attribute__((ext_vector_type(4)));
typedef unsigned u32x2 __attribute__((ext_vector_type(2)));
typedef __bf16 bf16x2_t __attribute__((ext_vector_type(2)));
typedef unsigned long long u64;
typedef __amdgpu_buffer_rsrc_t rsrc_t;
#define LAS __attribute__((address_space(3)))
#define DI __device__ __forceinline__
typedef LAS char* lptr;

constexpr int SEQ = 2048, DM = 448, DH = 28, FF = 1792;
constexpr float LOG2E = 1.4426950408889634f;
constexpr float QSCALE = 0.18898223650461362f * LOG2E;

constexpr unsigned WS_Q = 0;
constexpr unsigned WS_K = WS_Q + (2u << 20);
constexpr unsigned WS_V = WS_K + (2u << 20);
constexpr unsigned WS_O = WS_V + (2u << 20);
constexpr unsigned WS_H = WS_O + (2u << 20);
constexpr unsigned WS_G = WS_H + 64u * 112 * 1024;
constexpr unsigned WS_FQ = WS_G + 16384;
constexpr unsigned WS_FH = WS_FQ + 8192;
constexpr unsigned WS_FO = WS_FH + 16384;
constexpr unsigned WS_E = WS_FO + 8192;
constexpr unsigned WS_END = WS_E + 256;
constexpr unsigned CODE_PF = 53000;
constexpr int PSLEEP = 10, PAUSE = 7;
constexpr unsigned OOB = 0x7ffffff0u;

constexpr int LDS_BYTES = 160 * 1024;
constexpr unsigned RG(int i) { const int t[6] = {4, 0, 1, 3, 7, 2}; return t[i] * 16384; }
#define COMB(w) (((0x8596u >> (4 * (w))) & 15u) << 14)
constexpr unsigned L_CNT = 6 * 16384 + 12288;

#define MFMA(a, b, c) __builtin_amdgcn_mfma_f32_32x32x16_bf16((a), (b), (c), 0, 0, 0)
#define SBAR() __builtin_amdgcn_sched_barrier(0)
#define PIN(x) asm volatile("" : "+v"(x))
#define BARRIER() asm volatile("s_waitcnt lgkmcnt(0)\n\ts_barrier" ::: "memory")
template <int N> DI void waitv() { asm volatile("s_waitcnt vmcnt(%0)" :: "n"(N) : "memory"); }


DI rsrc_t mk_rsrc(const void* p, unsigned bytes) { return __builtin_amdgcn_make_buffer_rsrc((void*)p, (short)0, (int)bytes, 0x00020000); }
DI unsigned rfl(unsigned v) { return (unsigned)__builtin_amdgcn_readfirstlane((int)v); }
DI void dma16(rsrc_t r, unsigned voff, unsigned soff, unsigned ldsaddr) {
    asm volatile("s_mov_b32 m0, %0\n\ts_nop 0\n\tbuffer_load_dwordx4 %1, %2, %3 offen lds" :: "s"(ldsaddr), "v"(voff), "s"(r), "s"(soff) : "memory");
}
DI void dma4(rsrc_t r, unsigned voff, unsigned ldsaddr) {
    asm volatile("s_mov_b32 m0, %0\n\ts_nop 0\n\tbuffer_load_dword %1, %2, 0 offen lds" :: "s"(ldsaddr), "v"(voff), "s"(r) : "memory");
}
DI void gload16_sc1(u32x4& dst, rsrc_t r, unsigned voff, unsigned soff) {
    asm volatile("buffer_load_dwordx4 %0, %1, %2, %3 offen sc1" : "=v"(dst) : "v"(voff), "s"(r), "s"(soff) : "memory");
}
DI bf16x8 ld_sc1(rsrc_t r, unsigned voff, unsigned soff) { return __builtin_bit_cast(bf16x8, __builtin_amdgcn_raw_buffer_load_b128(r, voff, soff, 16)); }
DI void st_sc1(u32x4 v, rsrc_t r, unsigned voff) { __builtin_amdgcn_raw_buffer_store_b128(v, r, voff, 0, 16); }
DI unsigned cvtpk(float lo, float hi) { return __builtin_bit_cast(unsigned, __builtin_convertvector((f32x2){lo, hi}, bf16x2_t)); }
DI bf16x8 lds_b128(lptr p) { return *(const LAS bf16x8*)p; }
DI s16x4 lds_tr(lptr p) { return __builtin_amdgcn_ds_read_tr16_b64_v4i16((LAS s16x4*)p); }
DI bf16x8 cat(s16x4 lo, s16x4 hi) { return __builtin_shufflevector(lo, hi, 0, 1, 2, 3, 4, 5, 6, 7); }
DI u32x4 pack8(const f32x16& a, int s) {
    u32x4 r; r[0] = cvtpk(a[8 * s], a[8 * s + 1]); r[1] = cvtpk(a[8 * s + 2], a[8 * s + 3]); r[2] = cvtpk(a[8 * s + 4], a[8 * s + 5]); r[3] = cvtpk(a[8 * s + 6], a[8 * s + 7]); return r;
}
DI float sigmoidf_(float v) { return __builtin_amdgcn_rcpf(1.f + __builtin_amdgcn_exp2f(-LOG2E * v)); }

DI void set_flag(rsrc_t rws, unsigned off, u64 tag) {
    __builtin_amdgcn_raw_buffer_store_b64((u32x2){(unsigned)tag, (unsigned)(tag >> 32)}, rws, off, 0, 16);
}
template <int NW, int SLEEP = 4> DI void wait_flags(rsrc_t rws, unsigned off, u64 tag, unsigned lim = ~0u) {
#pragma unroll 1
    for (int spin = 0; spin < 40000; ++spin) {
        bool ok = true;
        asm volatile("" ::: "memory");
#pragma unroll
        for (int i = 0; i < NW; ++i) { const u32x2 v = __builtin_amdgcn_raw_buffer_load_b64(rws, off + i * 512, 0, 16); ok &= ((v[0] == (unsigned)tag) & (v[1] == (unsigned)(tag >> 32))) | (off + i * 512 - WS_FQ >= lim); }
        if (__all(ok)) break;
        __builtin_amdgcn_s_sleep(SLEEP);
    }
    asm volatile("" ::: "memory");
}
DI float load_gate(rsrc_t rws, unsigned tok, u64 tag) {
    u32x2 v;
#pragma unroll 1
    for (int spin = 0; spin < 40000; ++spin) {
        asm volatile("" ::: "memory");
        v = __builtin_amdgcn_raw_buffer_load_b64(rws, WS_G + tok * 8, 0, 16);
        if (__all(v[1] == (unsigned)tag)) break;
        __builtin_amdgcn_s_sleep(2);
    }
    return __uint_as_float(v[0]);
}
DI void lds_wait(lptr cnt, unsigned target) {
#pragma unroll 1
    for (int spin = 0; spin < 4000000; ++spin) {
        if (rfl(__hip_atomic_load((LAS unsigned*)cnt, __ATOMIC_RELAXED, __HIP_MEMORY_SCOPE_WORKGROUP)) >= target) break;
        __builtin_amdgcn_s_sleep(4);
    }
    asm volatile("" ::: "memory");
}
DI void group_barrier(lptr words, unsigned w) {
    asm volatile("s_waitcnt lgkmcnt(0)" ::: "memory");
    *(volatile LAS unsigned*)(words + w * 4) = 1u;
#pragma unroll 1
    for (int spin = 0; spin < 4000000; ++spin) {
        const u32x4 v = *(volatile LAS u32x4*)words;
        if (rfl(v[0] & v[1] & v[2] & v[3])) break;
    }
    asm volatile("" ::: "memory");
}

struct WSrc { rsrc_t r; unsigned v0, v1, step; };
DI WSrc mk_wsrc(rsrc_t r, unsigned pitch, unsigned colb, unsigned row0, unsigned rows_per_stage, int lane) {
    WSrc s; s.r = r; const unsigned sw = ((lane & 7) ^ (((lane >> 4) & 1) << 2)) << 4;
    s.v0 = (row0 + (lane >> 3)) * pitch + colb + sw; s.v1 = s.v0 + 8 * pitch; s.step = rows_per_stage * pitch; return s;
}
constexpr int OB_NS = 6, OB_P = 4;
struct OutC { rsrc_t rd, ro, rws; unsigned l0, vw, va, w, rb; };
DI void out_issueW1(const OutC& c, int i, int p) {
    const unsigned so = i < 14 ? (c.w * 448 + i * 32) * (DM * 2) : (c.w * 4 + (i - 14)) * DH * (DM * 2);
    dma16(i < 14 ? c.rd : c.ro, c.vw, so + p * 8 * (DM * 2), c.l0 + RG(i % OB_NS) + p * 1024);
}
DI void out_issueW(const OutC& c, int i) {
#pragma unroll
    for (int p = 0; p < 4; ++p) out_issueW1(c, i, p);
}
constexpr unsigned it_reg(int P, int it) { const int x[7] = {0, 3, 7, 8, 1, 2, 5}, q[4] = {6, 9, 4, 0}, f[14] = {1, 2, 4, 5, 3, 6, 9, 4, 0, 3, 7, 6, 9, 8};
    const int x1[7] = {0, 3, 6, 9, 2, 0, 3}, f1[14] = {1, 2, 4, 5, 7, 8, 1, 4, 5, 7, 8, 1, 4, 5};
    return (P ? (it < 10 ? x1[it] : f1[it - 20]) : (it < 10 ? x[it] : (it < 20 ? q[it - 10] : f[it - 20]))) * 16384; }
constexpr int it_first(int P, int it) { const int x[7] = {0, 2, 5, 5, 6, 6, 7};
    return it >= 40 ? 99 : (P ? (it < 10 ? 2 * it : it - 20) : (it < 10 ? x[it] : (it < 20 ? it - 6 : (it < 24 ? it - 20 : it - 16)))); }
constexpr int g_bar(int P, int g) { return (P || g <= 13) ? (((g & 1) && g < 27) ? (g + 1) / 2 : -1) : ((g & 1) || g > 32 ? -1 : (g + 2) / 2); }
constexpr int g_item(int P, int g, int u) {
    const int t[23] = {22, 10, 2, 3, 11, 4, 5, 12, 6, 13, 24, 25, 26, 27, 28, -1, 29, -1, 30, 31, 32, -1, 33};
    const int t1[23] = {22, 2, 24, 25, 3, 26, 4, 27, 5, 28, 29, 6, 30, 31, -1, 32, -1, 33, -1, -1, -1, -1, -1};
    return u ? (g == 0 ? 23 : -1) : (g < 23 ? (P ? t1[g] : t[g]) : -1); }
constexpr int g_dd(int P, int H, int g) { return (H == 1 && !P) ? (g == 23 ? 0 : (g == 24 ? 1 : (g == 26 ? 2 : (g == 27 ? 3 : (g == 28 ? 4 : (g == 30 ? 5 : -1)))))) : -1; }
constexpr int g_st(int P, int g) { return (!P && g >= 16 && g < 19) ? g - 16 : -1; }
constexpr int ST_PUB = 13;
constexpr int vm_count(int P, int H, int gs) {
    int tag[96] = {}, cnt[96] = {}, m = 0; const int n = gs < 0 ? 0 : g_bar(P, gs);
    { const int t[4] = {0, 20, 21, 1}; for (int i = 0; i < 4; ++i) { tag[m] = t[i]; cnt[m++] = 2; } }
    for (int g = 0; g < gs; ++g) {
        for (int u = 0; u < 2; ++u) if (g_item(P, g, u) >= 0) { tag[m] = g_item(P, g, u); cnt[m++] = 2; }
        if (g_dd(P, H, g) >= 0) { tag[m] = 40; cnt[m++] = 4; }
        if (g_st(P, g) >= 0) { tag[m] = 98; cnt[m++] = 1; }
    }
    int last = -1;
    for (int i = 0; i < m; ++i) if (it_first(P, tag[i]) <= n || (tag[i] == 98 && n == ST_PUB)) last = i;
    int r = 0; for (int i = last + 1; i < m; ++i) r += cnt[i];
    return r;
}
struct P1 { lptr lds; unsigned l0, xsw; int lane; WSrc sx, sq, sf; OutC oc; unsigned xf[3], wf[3], xq[3], wq[3]; rsrc_t rws; u64 tag; u32x4 sd[3]; unsigned so[3], fqo, pollo; u32x2 fq; unsigned rot, qh; };
template <int P, int IT> DI void issue1(const P1& c, int p) {
    const WSrc& s = IT < 10 ? c.sx : (IT < 20 ? c.sq : c.sf);
    unsigned sq = (IT >= 10 && IT < 20) ? c.rot >> (4 * c.qh) : c.rot; asm volatile("" : "+s"(sq));
    const unsigned ch = (sq >> (4 * (IT < 10 ? IT : (IT < 20 ? 2 * (IT - 10) : (IT - 20) >> 1)))) & 7;
    dma16(s.r, p ? s.v1 : s.v0, (IT >= 20 ? 2 * ch + (IT & 1) : ch) * s.step, c.l0 + it_reg(P, IT) + p * 1024);
}
template <int IT> DI void issue2(const P1& c) { issue1<0, IT>(c, 0); issue1<0, IT>(c, 1); }
struct Frags { bf16x8 x[4], w[4]; };
DI lptr at(const P1& c, const unsigned (&b)[3], unsigned off) { return c.lds + b[off >> 16] + (off & 65535); }
template <int P, int G> DI void rd_part(Frags& f, const P1& c, int i) {
    if constexpr (P || G < 8 || G >= 15) {
        constexpr int K = (P || G < 8) ? G : G - 7;
        f.x[i] = lds_b128(at(c, c.xf, it_reg(P, K >> 2) + i * 4096) + (((2 * (K & 3)) ^ c.xsw) << 4));
        lptr wp = at(c, c.wf, it_reg(P, 20 + (K >> 1)) + (K & 1) * 2048 + i * 4096);
        f.w[i] = cat(lds_tr(wp), lds_tr(wp + 512));
    } else {
#pragma unroll
        for (int u = 2 * i; u < 2 * i + 2; ++u) {
            f.x[u] = lds_b128(at(c, c.xq, it_reg(0, G - 8)) + (((2 * u) ^ c.xsw) << 4));
            lptr wp = at(c, c.wq, it_reg(0, 10 + ((G - 8) >> 1)) + ((G & 1) * 4 + u) * 2048);
            f.w[u] = cat(lds_tr(wp), lds_tr(wp + 512));
        }
    }
}
template <int H, int G, int MODE> DI void group(P1& c, Frags& fc, Frags& fn, f32x16 (&acc)[4], f32x16& aq) {
    constexpr int P = MODE == 2; constexpr bool NEXT = G + 1 < (P ? 28 : 35), QG = !P && G >= 8 && G < 15;
    constexpr int I0 = g_item(P, G, 0), I1 = g_item(P, G, 1), DD = g_dd(P, H, G), ST = g_st(P, G);
    if constexpr (g_bar(P, G) >= 0) {
        waitv<vm_count(P, H, G)>(); BARRIER();
        if constexpr (H == 0 && g_bar(P, G) == ST_PUB) set_flag(c.rws, c.fqo, c.tag);
        if constexpr (H == 0 && G == 32) asm volatile("buffer_load_dwordx2 %0, %1, %2, 0 offen sc1" : "=v"(c.fq) : "v"(c.pollo), "s"(c.rws) : "memory");
    }
    if constexpr (H == 1 && G == 31) { if (c.lane < 32) *(LAS unsigned*)(c.lds + L_CNT + c.lane * 4) = 0; }
    SBAR();
#pragma unroll
    for (int i = 0; i < 4; ++i) {
        if constexpr (!QG) acc[i] = MFMA(fc.w[i & 1], fc.x[i >> 1], acc[i]);
        else if constexpr (MODE == 1) aq = MFMA(fc.x[i], fc.w[i], aq);
        else if constexpr (MODE == 0) aq = MFMA(fc.w[i], fc.x[i], aq);
        if constexpr (NEXT) { if (i < 2) rd_part<P, NEXT ? G + 1 : G>(fn, c, i); }
        SBAR();
        if constexpr (I1 >= 0) { if (i < 2) issue1<P, I0 >= 0 ? I0 : 0>(c, i); else issue1<P, I1 >= 0 ? I1 : 0>(c, i - 2); SBAR(); }
        else if constexpr (I0 >= 0) { if ((i >> 1) == H) { issue1<P, I0 >= 0 ? I0 : 0>(c, i & 1); SBAR(); } }
        if constexpr (DD >= 0) { out_issueW1(c.oc, DD >= 0 ? DD : 0, i); SBAR(); }
        if constexpr (ST >= 0) { if (i == 2 * H + 1) { st_sc1(c.sd[ST >= 0 ? ST : 0], c.rws, c.so[ST >= 0 ? ST : 0]); SBAR(); } }
    }
}
template <int H, int G0, int G1, int MODE> DI void groups(P1& c, Frags& fa, Frags& fb, f32x16 (&acc)[4], f32x16& aq) {
    if constexpr (G0 < G1) {
        if constexpr (G0 & 1) group<H, G0, MODE>(c, fb, fa, acc, aq); else group<H, G0, MODE>(c, fa, fb, acc, aq);
        groups<H, G0 + 1, G1, MODE>(c, fa, fb, acc, aq);
    }
}
DI void epi_qk(const f32x16& a, float sc, u32x4* sd) {
#pragma unroll
    for (int p = 0; p < 2; ++p) {
        unsigned a0 = cvtpk(a[8 * p] * sc, a[8 * p + 1] * sc), a1 = cvtpk(a[8 * p + 2] * sc, a[8 * p + 3] * sc), b0 = cvtpk(a[8 * p + 4] * sc, a[8 * p + 5] * sc), b1 = cvtpk(a[8 * p + 6] * sc, a[8 * p + 7] * sc);
        auto s0 = __builtin_amdgcn_permlane32_swap(a0, b0, false, false); auto s1 = __builtin_amdgcn_permlane32_swap(a1, b1, false, false);
        sd[p] = (u32x4){s0[0], s1[0], s0[1], s1[1]};
    }
}
DI void epi_h(const f32x16& g, const f32x16& u, u32x4* d) {
    f32x16 hv;
#pragma unroll
    for (int i = 0; i < 16; ++i) hv[i] = u[i] * sigmoidf_(g[i]);
    d[0] = pack8(hv, 0); d[1] = pack8(hv, 1);
}
DI void store_h(const u32x4 (&d)[4], unsigned o, rsrc_t rws) {
#pragma unroll
    for (int i = 0; i < 4; ++i) st_sc1(d[i], rws, o + (i >> 1) * 112 * 1024 + (i & 1) * 1024);
}
template <int H> DI void phase1(P1& c, f32x16 (&acc)[4], unsigned cg, unsigned rb, unsigned wid) {
    Frags fa, fb; f32x16 aq = {};
    const int lane = c.lane; const rsrc_t rws = c.rws;
#pragma unroll
    for (int i = 0; i < 4; ++i) acc[i] = (f32x16){};
    waitv<vm_count(0, H, -1)>(); BARRIER();
    const unsigned rp = wid & 1, pi = wid >> 1, tt = rb * 4 + (wid & 3);
    if (cg >= 14) {
        asm volatile("" ::: "memory");
        rd_part<0, 0>(fa, c, 0); rd_part<0, 0>(fa, c, 1);
        groups<H, 0, 28, 2>(c, fa, fb, acc, aq);
#pragma unroll
        for (int i = 0; i < 4; ++i) { u32x4 sd[2]; const unsigned kb = 4 * cg - 56 + H * 2 + (i & 1);
            const unsigned o = (kb < 7 ? WS_K : OOB) + kb * 4096 + (pi & 1) * 2048 + (rb * 4 + 2 * rp + (i >> 1)) * 28672 + lane * 16;
            epi_qk(acc[i], 1.f, sd); st_sc1(sd[0], rws, o); st_sc1(sd[1], rws, o + 1024); }
        if constexpr (H == 1) {
#pragma unroll
            for (int t = 0; t < 2; ++t) __builtin_amdgcn_raw_buffer_store_b64((u32x2){__float_as_uint(sigmoidf_(acc[2 * t + 1][0] + acc[2 * t + 1][5])), (unsigned)c.tag}, rws,
                                                                              (cg == 15 && pi == 2 && lane < 32) ? WS_G + ((rb * 4 + 2 * rp + t) * 32 + lane) * 8 : OOB, 0, 16);
        }
        if (H == 1) { if (lane < 32) *(LAS unsigned*)(c.lds + L_CNT + lane * 4) = 0; }
        waitv<0>();
        BARRIER();
        if (H == 0) { c.fq = (u32x2){0, 0}; set_flag(rws, (wid == 0 && lane < 4 && 4 * cg - 56 + lane < 7) ? WS_FQ + ((4 * cg - 49 + lane) * 16 + rb) * 8 : OOB, c.tag); }
        else {
#pragma unroll
            for (int i = 0; i < OB_NS; ++i) out_issueW(c.oc, i);
        }
        return;
    }
    asm volatile("" ::: "memory");
    rd_part<0, 0>(fa, c, 0); rd_part<0, 0>(fa, c, 1);
    groups<H, 0, 8, 0>(c, fa, fb, acc, aq);
    c.sd[2] = (u32x4){0x3f803f80u, 0x3f803f80u, 0x3f803f80u, 0x3f803f80u}; c.so[2] = OOB;
    if (cg < 7) {
        groups<H, 8, 16, 1>(c, fa, fb, acc, aq);
        const unsigned n = cg * 64 + H * 32 + (lane & 31), hd = n / 28, ph = (cg * 64 + 27) / 28 + (lane >> 2);
        c.sd[0] = pack8(aq, 0); c.sd[1] = pack8(aq, 1);
        c.so[0] = WS_V + (hd * 64 + tt) * 2048 + (n - 28 * hd + (lane & 32)) * 16;
        if (H == 0) c.so[2] = ph * 28 < (cg + 1) * 64 ? WS_V + (ph * 64 + tt) * 2048 + (lane & 1) * 1024 + (28 + 16 * (lane & 2)) * 16 : OOB;
    } else {
        groups<H, 8, 16, 0>(c, fa, fb, acc, aq);
        epi_qk(aq, QSCALE, c.sd);
        c.so[0] = WS_Q + (cg - 7) * 4096 + H * 2048 + tt * 28672 + lane * 16;
    }
    c.so[1] = c.so[0] + 1024;
    groups<H, 16, 35, 0>(c, fa, fb, acc, aq);
    if constexpr (H == 0) asm volatile("s_waitcnt vmcnt(0)" : "+v"(c.fq) :: "memory");
    BARRIER();
}

DI float max16(const f32x16& p) {
    float a = fmaxf(p[0], p[1]);
#pragma unroll
    for (int i = 2; i < 16; ++i) a = fmaxf(a, p[i]);
    auto rr = __builtin_amdgcn_permlane32_swap(__float_as_uint(a), __float_as_uint(a), false, false);
    return fmaxf(__uint_as_float(rr[0]), __uint_as_float(rr[1]));
}
#define EXP2(v) __builtin_amdgcn_exp2f(v)
DI void attn_part(char* ws, rsrc_t rws, lptr lds, unsigned wid, int lane, unsigned h, unsigned rb, u64 tag, unsigned ho, const f32x16 (&acc)[4], bool ready, unsigned pollo) {
    const unsigned hh = lane >> 5, va = lane * 16, vv = (lane & 31) > 28 ? OOB : va;
    const unsigned c0 = (7 * h >> 1) * 512;
    const rsrc_t rk = mk_rsrc(ws + WS_K + c0, 64 * 28672 - c0), rv = mk_rsrc(ws + WS_V + h * (64 * 2048), 64 * 2048);
    const unsigned kb = wid * (16 * 28672), vb = wid * (16 * 2048);
    bf16x8 q[4][2], kv[4][4];
    auto ldq = [&](unsigned t, int s) {
        u32x4 v = __builtin_bit_cast(u32x4, ld_sc1(rws, va, WS_Q + c0 + t * 28672 + s * 1024));
        const bool z = s ? (!(h & 1) && hh) : ((h & 1) && !hh);
        v[2 * s] = z ? 0u : v[2 * s]; v[2 * s + 1] = z ? 0u : v[2 * s + 1]; return __builtin_bit_cast(bf16x8, v); };
    auto ldkv = [&](bf16x8 (&d)[4], unsigned t) {
        d[0] = ld_sc1(rk, va, kb + t * 28672); d[1] = ld_sc1(rk, va, kb + t * 28672 + 1024); d[2] = ld_sc1(rv, vv, vb + t * 2048); d[3] = ld_sc1(rv, vv, vb + t * 2048 + 1024); };
    u32x4 qraw[4][2]; bf16x8 kz0, kz1;
    auto first_loads = [&]() {
#pragma unroll
        for (int i = 0; i < 4; ++i) { const unsigned qo = WS_Q + c0 + (rb * 4 + ((wid + 1 + i) & 3)) * 28672;
            qraw[i][0] = __builtin_bit_cast(u32x4, ld_sc1(rws, va, qo)); qraw[i][1] = __builtin_bit_cast(u32x4, ld_sc1(rws, va, qo + 1024)); }
        kz0 = ld_sc1(rk, va, 0); kz1 = ld_sc1(rk, va, 1024);
#pragma unroll
        for (int t = 0; t < 4; ++t) ldkv(kv[t], t);
    };
    u32x2 f2 = {0, 0};
    if (__builtin_expect(ready, 1)) first_loads(); else f2 = __builtin_amdgcn_raw_buffer_load_b64(rws, pollo, 0, 16);
    SBAR();
    u32x4 hd[4]; epi_h(acc[0], acc[1], hd); epi_h(acc[2], acc[3], hd + 2);
    PIN(hd[0]); PIN(hd[1]); PIN(hd[2]); PIN(hd[3]);
    if (__builtin_expect(!ready, 0)) { if (!__all((f2[0] == (unsigned)tag) & (f2[1] == (unsigned)(tag >> 32)))) wait_flags<1>(rws, pollo, tag); asm volatile("" ::: "memory"); first_loads(); }
    asm volatile("" ::: "memory");
    *(volatile LAS unsigned*)(lds + L_CNT + 32 + wid * 4) = 1u;
    store_h(hd, ho, rws);
    SBAR();
    const f32x16 zero = {};
    float mx = -INFINITY;
#pragma unroll
    for (int i = 0; i < 4; ++i) {
        const bool z0 = (h & 1) && !hh, z1 = !(h & 1) && hh;
        qraw[i][0][0] = z0 ? 0u : qraw[i][0][0]; qraw[i][0][1] = z0 ? 0u : qraw[i][0][1]; qraw[i][1][2] = z1 ? 0u : qraw[i][1][2]; qraw[i][1][3] = z1 ? 0u : qraw[i][1][3];
        q[i][0] = __builtin_bit_cast(bf16x8, qraw[i][0]); q[i][1] = __builtin_bit_cast(bf16x8, qraw[i][1]);
        f32x16 s = MFMA(kz0, q[i][0], zero); s = MFMA(kz1, q[i][1], s);
#pragma unroll
        for (int r = 0; r < 16; ++r) mx = fmaxf(mx, s[r]);
    }
    { auto rr = __builtin_amdgcn_permlane32_swap(__float_as_uint(mx), __float_as_uint(mx), false, false); mx = -fmaxf(__uint_as_float(rr[0]), __uint_as_float(rr[1])); }
    f32x16 cm;
#pragma unroll
    for (int r = 0; r < 16; ++r) cm[r] = mx;
    PIN(cm);
    f32x16 o[4], sA, sB; u32x4 p0, p1 = {0, 0, 0, 0};
#pragma unroll
    for (int i = 0; i < 4; ++i) o[i] = zero;
    sA = MFMA(kv[0][0], q[0][0], cm); sA = MFMA(kv[0][1], q[0][1], sA);
    u32x2 gv;
    float tp0 = 0.f, tp1 = 0.f, tp2 = 0.f, tp3 = 0.f;
    auto wr_o = [&](int i) { const unsigned own = (wid + 1 + i) & 3; lptr d = lds + COMB(own) + i * 4096 + va;
#pragma unroll
        for (int g = 0; g < 4; ++g) *(LAS f32x4*)(d + g * 1024) = (f32x4){o[i][4 * g], o[i][4 * g + 1], o[i][4 * g + 2], o[i][4 * g + 3]}; };
#define UNIT(SC, SN, I, KN0, KN1, IN, V0, VP1, IP, LOADS) do { \
    SN = MFMA(KN0, q[IN][0], cm); SBAR(); p1[2] = cvtpk(tp0, tp1); p1[3] = cvtpk(tp2, tp3); PIN(p1); SBAR(); SC[0] = EXP2(SC[0]); SC[1] = EXP2(SC[1]); SC[2] = EXP2(SC[2]); SC[3] = EXP2(SC[3]); PIN(SC); SBAR(); \
    SN = MFMA(KN1, q[IN][1], SN); SBAR(); SC[4] = EXP2(SC[4]); SC[5] = EXP2(SC[5]); p0[0] = cvtpk(SC[0], SC[1]); SC[6] = EXP2(SC[6]); SC[7] = EXP2(SC[7]); p0[1] = cvtpk(SC[2], SC[3]); PIN(SC); PIN(p0); SBAR(); \
    o[IP] = MFMA(VP1, __builtin_bit_cast(bf16x8, p1), o[IP]); SBAR(); p0[2] = cvtpk(SC[4], SC[5]); p0[3] = cvtpk(SC[6], SC[7]); PIN(p0); SBAR(); \
    SC[8] = EXP2(SC[8]); SC[9] = EXP2(SC[9]); SC[10] = EXP2(SC[10]); SC[11] = EXP2(SC[11]); PIN(SC); SBAR(); \
    LOADS; \
    o[I] = MFMA(V0, __builtin_bit_cast(bf16x8, p0), o[I]); SBAR(); tp0 = EXP2(SC[12]); tp1 = EXP2(SC[13]); p1[0] = cvtpk(SC[8], SC[9]); tp2 = EXP2(SC[14]); tp3 = EXP2(SC[15]); p1[1] = cvtpk(SC[10], SC[11]); \
    PIN(tp0); PIN(tp1); PIN(tp2); PIN(tp3); PIN(p1); SBAR(); } while (0)
#define TILE(T) do { \
    UNIT(sA, sB, 0, kv[(T) & 3][0], kv[(T) & 3][1], 1, kv[(T) & 3][2], kv[(T) ? ((T) + 3) & 3 : 0][3], 3, { if ((T) > 0 && (T) + 3 < 16) { ldkv(kv[((T) + 3) & 3], (T) + 3); SBAR(); } }); \
    UNIT(sB, sA, 1, kv[(T) & 3][0], kv[(T) & 3][1], 2, kv[(T) & 3][2], kv[(T) & 3][3], 0, {}); \
    UNIT(sA, sB, 2, kv[(T) & 3][0], kv[(T) & 3][1], 3, kv[(T) & 3][2], kv[(T) & 3][3], 1, { if ((T) == 15) { wr_o(0); SBAR(); } }); \
    UNIT(sB, sA, 3, kv[((T) + 1) & 3][0], kv[((T) + 1) & 3][1], 0, kv[(T) & 3][2], kv[(T) & 3][3], 2, { if ((T) == 15) { wr_o(1); SBAR(); } }); } while (0)
    TILE(0); TILE(1);
    waitv<4>(); *(volatile LAS unsigned*)(lds + L_CNT + 64 + wid * 4) = 1u; SBAR();
    TILE(2); TILE(3); TILE(4); TILE(5); TILE(6); TILE(7); TILE(8); TILE(9); TILE(10); TILE(11); TILE(12);
    gv = __builtin_amdgcn_raw_buffer_load_b64(rws, WS_G + (rb * 128 + wid * 32 + (lane & 31)) * 8, 0, 16); SBAR();
    TILE(13); TILE(14);
    *(volatile LAS unsigned*)(lds + L_CNT + 48 + wid * 4) = 1u;
    __builtin_amdgcn_s_setprio(2); SBAR();
    TILE(15);
#undef TILE
#undef UNIT
    p1[2] = cvtpk(tp0, tp1); p1[3] = cvtpk(tp2, tp3);
    o[3] = MFMA(kv[3][3], __builtin_bit_cast(bf16x8, p1), o[3]);
    wr_o(2);
    PIN(o[3]);
    group_barrier(lds + L_CNT, wid);
    f32x16 r = o[3];
#pragma unroll
    for (int j = 0; j < 3; ++j)
#pragma unroll
        for (int g = 0; g < 4; ++g) { const f32x4 v = *(LAS f32x4*)(lds + COMB(wid) + (j * 4 + g) * 1024 + va); r[4 * g] += v[0]; r[4 * g + 1] += v[1]; r[4 * g + 2] += v[2]; r[4 * g + 3] += v[3]; }
    const unsigned tt = rb * 4 + wid, oo = WS_O + (tt * 16 + h) * 2048 + va, fo = lane == 0 ? WS_FO + ((tt >> 1) * 32 + h * 2 + (tt & 1)) * 8 : OOB;
    auto finish = [&](f32x16 v, float gtok) {
        auto rr = __builtin_amdgcn_permlane32_swap(__float_as_uint(v[12]), __float_as_uint(v[12]), false, false);
        const float inv = gtok * __builtin_amdgcn_rcpf(__uint_as_float(rr[1]));
#pragma unroll
        for (int i = 0; i < 16; ++i) v[i] = (hh && i >= 12) ? 0.f : v[i] * inv;
        st_sc1(pack8(v, 0), rws, oo); st_sc1(pack8(v, 1), rws, oo + 1024);
        waitv<0>();
        set_flag(rws, fo, tag);
    };
    bool ok = true;
#pragma unroll
    for (int i = 0; i < 16; ++i) ok &= fabsf(r[i]) < 1.8e19f;
    ok &= !hh || r[12] > 5.4e-20f;
    if (__builtin_expect(__all(ok && gv[1] == (unsigned)tag), 1)) { finish(r, __uint_as_float(gv[0])); return; }
    if (!__all(ok)) {
        wait_flags<6>(rws, WS_FQ + lane * 8, tag, 21 * 128);
        const bf16x8 q0 = ldq(rb * 4 + wid, 0), q1 = ldq(rb * 4 + wid, 1);
        float m = -INFINITY; r = zero;
#pragma unroll 1
        for (int t = 0; t < 64; ++t) {
            const bf16x8 k0 = ld_sc1(rk, va, t * 28672), k1 = ld_sc1(rk, va, t * 28672 + 1024), v0 = ld_sc1(rv, vv, t * 2048), v1 = ld_sc1(rv, vv, t * 2048 + 1024);
            f32x16 s = MFMA(k0, q0, zero); s = MFMA(k1, q1, s);
            const float mn = fmaxf(m, max16(s)), al = EXP2(m - mn); m = mn;
#pragma unroll
            for (int i = 0; i < 16; ++i) { s[i] = EXP2(s[i] - mn); r[i] *= al; }
            r = MFMA(v0, __builtin_bit_cast(bf16x8, pack8(s, 0)), r); r = MFMA(v1, __builtin_bit_cast(bf16x8, pack8(s, 1)), r);
        }
    }
    asm volatile("; slow path" ::: "memory");
    finish(r, load_gate(rws, rb * 128 + wid * 32 + (lane & 31), tag));
}

DI void out_issueA(const OutC& c, int i, u32x4 (&a)[4]) {
#pragma unroll
    for (int t = 0; t < 2; ++t)
#pragma unroll
        for (int s = 0; s < 2; ++s)
            gload16_sc1(a[2 * t + s], c.rws, c.va, i < 14 ? WS_H + ((c.rb * 2 + (t ^ (c.w >> 1))) * 112 + c.w * 28 + 2 * i + s) * 1024 : WS_O + (((c.rb * 2 + (t ^ (c.w >> 1))) * 16 + c.w * 4 + (i - 14)) * 2 + s) * 1024);
}
template <int N> DI void waitv4(u32x4 (&a)[4]) { asm volatile("s_waitcnt vmcnt(%4)" : "+v"(a[0]), "+v"(a[1]), "+v"(a[2]), "+v"(a[3]) : "n"(N) : "memory"); }
constexpr int out_younger(int i) { int n = 0;
    for (int k = i + 1; k < OB_P; ++k) n += 4;
    for (int j = (i - OB_P + 1 > 0 ? i - OB_P + 1 : 0); j < i; ++j) { if (j + OB_NS < 18) n += 4; if (j + OB_P < 14) n += 4; }
    return n; }
template <int I> DI void out_stage(const OutC& c, lptr lds, const unsigned (&woff)[3][2], u32x4 (&af)[OB_P][4], f32x16 (&acc)[2][2]) {
    u32x4(&a)[4] = af[I % OB_P];
    waitv4<out_younger(I)>(a);
    lptr sl = lds + (RG(I % OB_NS) & 65535);
#pragma unroll
    for (int s = 0; s < 2; ++s) {
        bf16x8 wf[2];
#pragma unroll
        for (int cc = 0; cc < 2; ++cc) wf[cc] = cat(lds_tr(sl + woff[RG(I % OB_NS) >> 16][cc] + s * 2048), lds_tr(sl + woff[RG(I % OB_NS) >> 16][cc] + s * 2048 + 1024));
#pragma unroll
        for (int t = 0; t < 2; ++t)
#pragma unroll
            for (int cc = 0; cc < 2; ++cc) acc[t][cc] = MFMA(wf[cc], __builtin_bit_cast(bf16x8, a[2 * t + s]), acc[t][cc]);
    }
    SBAR();
    if constexpr (I + OB_NS < 18) out_issueW(c, I + OB_NS);
    if constexpr (I + OB_P < 14) out_issueA(c, I + OB_P, a);
}
template <int I0, int I1> DI void out_stages(const OutC& c, lptr lds, const unsigned (&woff)[3][2], u32x4 (&af)[OB_P][4], f32x16 (&acc)[2][2]) {
    if constexpr (I0 < I1) { out_stage<I0>(c, lds, woff, af, acc); out_stages<I0 + 1, I1>(c, lds, woff, af, acc); }
}
DI void out_part(const OutC& c, char* ws, uint16_t* out, lptr lds, int lane, unsigned nb, u64 tag) {
    const unsigned r32 = lane & 31, hh = lane >> 5, w = c.w, rb = c.rb;
    u32x4 af[OB_P][4];
    const u32x2 gv0 = __builtin_amdgcn_raw_buffer_load_b64(c.rws, WS_G + (rb * 64 + (w >> 1) * 32 + r32) * 8, 0, 16), gv1 = __builtin_amdgcn_raw_buffer_load_b64(c.rws, WS_G + (rb * 64 + ((w >> 1) ^ 1) * 32 + r32) * 8, 0, 16);
    if (w == 0) {
        __builtin_amdgcn_s_sleep(PSLEEP);
        wait_flags<1, 10>(c.rws, WS_FH + ((rb >> 1) * 16 + ((lane & 15) < 14 ? lane & 15 : 0)) * 8, tag);
        *(volatile LAS unsigned*)(lds + L_CNT + 96) = 1u;
    } else lds_wait(lds + L_CNT + 96, 1);
#pragma unroll
    for (int i = 0; i < OB_P; ++i) out_issueA(c, i, af[i]);
    f32x16 ab[2][2] = {};
    const unsigned q = (lane & 15) >> 2;
    unsigned woff[3][2];
#pragma unroll
    for (int cc = 0; cc < 2; ++cc)
#pragma unroll
        for (int u = 0; u < 3; ++u) { woff[u][cc] = u * 65536 + w * 4096 + (4 * hh + q) * 128 + ((cc ^ (w & 1) ^ (q >> 1)) << 6) + ((lane >> 4) & 1) * 32 + (lane & 3) * 8; PIN(woff[u][cc]); }
    __builtin_amdgcn_s_setprio(0);
    out_stages<0, PAUSE>(c, lds, woff, af, ab);
    if (PAUSE < 14) {
#pragma unroll 1
        for (int spin = 0; spin < 100000; ++spin) { if (rfl(*(volatile LAS unsigned*)(lds + L_CNT + 48 + w * 4))) break; __builtin_amdgcn_s_sleep(8); }
        asm volatile("" ::: "memory");
    }
    out_stages<PAUSE, 14>(c, lds, woff, af, ab);
    __builtin_amdgcn_s_setprio(1);
    {
        float g0 = __uint_as_float(gv0[0]), g1 = __uint_as_float(gv1[0]);
        if (__builtin_expect(!__all((gv0[1] == (unsigned)tag) & (gv1[1] == (unsigned)tag)), 0)) { g0 = load_gate(c.rws, rb * 64 + (w >> 1) * 32 + r32, tag); g1 = load_gate(c.rws, rb * 64 + ((w >> 1) ^ 1) * 32 + r32, tag); }
#pragma unroll
        for (int cc = 0; cc < 2; ++cc) { ab[0][cc] *= 1.f - g0; ab[1][cc] *= 1.f - g1; PIN(ab[0][cc]); PIN(ab[1][cc]); }
    }
    bf16x8 wo_f[4][2][2];
    waitv<0>();
#pragma unroll
    for (int i = 0; i < 4; ++i)
#pragma unroll
        for (int s = 0; s < 2; ++s)
#pragma unroll
            for (int cc = 0; cc < 2; ++cc) { lptr p = lds + (RG((14 + i) % OB_NS) & 65535) + woff[RG((14 + i) % OB_NS) >> 16][cc] + s * 2048; wo_f[i][s][cc] = cat(lds_tr(p), lds_tr(p + 1024)); PIN(wo_f[i][s][cc]); }
    wait_flags<1>(c.rws, WS_FO + (rb * 32 + w * 8 + (lane & 7)) * 8, tag);
#pragma unroll
    for (int i = 0; i < 4; ++i) out_issueA(c, 14 + i, af[i]);
#define OSTAGE(i) do { waitv4<(3 - (i)) * 4>(af[i]); \
    _Pragma("unroll") for (int s = 0; s < 2; ++s) _Pragma("unroll") for (int l = 3; l >= 0; --l) ab[l >> 1][l & 1] = MFMA(wo_f[i][s][l & 1], __builtin_bit_cast(bf16x8, af[i][2 * (l >> 1) + s]), ab[l >> 1][l & 1]); SBAR(); } while (0)
    OSTAGE(0); OSTAGE(1); OSTAGE(2); OSTAGE(3);
#undef OSTAGE
#pragma unroll
    for (int l = 3; l >= 1; --l)
#pragma unroll
        for (int g = 0; g < 4; ++g)
            *(LAS f32x4*)(lds + RG(l) + w * 4096 + g * 1024 + lane * 16) = (f32x4){ab[l >> 1][l & 1][4 * g], ab[l >> 1][l & 1][4 * g + 1], ab[l >> 1][l & 1][4 * g + 2], ab[l >> 1][l & 1][4 * g + 3]};
    PIN(ab[0][0]);
    group_barrier(lds + L_CNT + 16, w);
    f32x16 r = ab[0][0];
#pragma unroll
    for (int l = 1; l < 4; ++l)
#pragma unroll
        for (int g = 0; g < 4; ++g) { const f32x4 v = *(LAS f32x4*)(lds + RG(l) + (w ^ l) * 4096 + g * 1024 + lane * 16); r[4 * g] += v[0]; r[4 * g + 1] += v[1]; r[4 * g + 2] += v[2]; r[4 * g + 3] += v[3]; }
    u32x4 sd[2]; epi_qk(r, 1.f, sd);
    const rsrc_t ro = mk_rsrc(out, SEQ * DM * 2); const unsigned oo = ((rb * 64 + (w >> 1) * 32 + r32) * DM + nb * 64 + (w & 1) * 32 + 8 * hh) * 2;
    __builtin_amdgcn_raw_buffer_store_b128(sd[0], ro, oo, 0, 0); __builtin_amdgcn_raw_buffer_store_b128(sd[1], ro, oo + 32, 0, 0);
}

__global__ __launch_bounds__(512, 2) void fused(const uint16_t* x, const uint16_t* wqkv, const uint16_t* wo, const uint16_t* wg, const uint16_t* wu,
                                                const uint16_t* wd, const uint16_t* gw, char* ws, uint16_t* out) {
    extern __shared__ __attribute__((aligned(16))) char smem[];
    lptr lds = (lptr)smem;
    u64 pc; asm volatile("s_getpc_b64 %0" : "=s"(pc));
    const int tid = threadIdx.x, lane = tid & 63; const unsigned wid = rfl(tid >> 6);
    const unsigned b = blockIdx.x, xc = b & 7, j = b >> 3;
    dma4(mk_rsrc((const void*)pc, CODE_PF), lane < 2 ? (lane * 256 + j * 8 + wid) * 128 : OOB, (unsigned)(uintptr_t)lds + wid * 2048 + it_reg(0, 22));
    SBAR();
    u64 tag;
    const rsrc_t rws = mk_rsrc(ws, WS_END);
    OutC oc; oc.rd = mk_rsrc(wd, FF * DM * 2); oc.ro = mk_rsrc(wo, DM * DM * 2); oc.rws = rws; oc.w = wid & 3; oc.rb = xc * 4 + (j & 3);
    oc.l0 = (unsigned)(uintptr_t)lds + oc.w * 4096; oc.va = lane * 16;
    oc.vw = (lane >> 3) * (DM * 2) + (j < 28 ? (j >> 2) * 128 : OOB) + (((lane & 7) ^ (((lane >> 4) & 1) << 2)) << 4);
    const unsigned rb = 8 * (xc & 1) + (j & 7), cg = 4 * (j >> 3) + (xc >> 1);
    const unsigned rp = wid & 1, pi = wid >> 1, rt = wid & 3, half = wid >> 2;
    const unsigned h = 2 * xc + (j & 1), ab = j >> 1;
    P1 c; c.lds = lds; c.l0 = (unsigned)(uintptr_t)lds + wid * 2048; c.lane = lane; c.oc = oc; c.rws = rws; c.qh = half;
    c.rot = (unsigned)((0x5432106543210ull >> (4 * (((0x65432106543210ull >> (4 * ((j & 7) + 2 * (j >> 3)))) & 7)))));
    {
        const unsigned xr = 16 * wid + (lane >> 3), qb = cg < 7 ? 14 + cg : cg - 7;
        c.sx.r = mk_rsrc(x, SEQ * DM * 2); c.sx.step = 128;
        c.sx.v0 = (rb * 128 + xr) * (DM * 2) + (((lane & 7) ^ ((xr >> 1) & 7)) << 4); c.sx.v1 = (rb * 128 + xr + 8) * (DM * 2) + (((lane & 7) ^ (((xr + 8) >> 1) & 7)) << 4);
        issue2<0>(c);
        const bool ff = cg < 14; const unsigned pitch = ff ? FF * 2 : 3 * DM * 2, a0 = 2 * cg + half, a1 = 4 * cg - 49 + pi, fcol = (ff ? a0 : a1) * 128;
        c.sf = mk_wsrc(mk_rsrc(ff ? ((wid & 2) ? wu : wg) : wqkv, DM * pitch), pitch, fcol, 16 * rp, 32, lane);
        if (__builtin_expect(cg == 15 && pi == 3, 0)) {
            const unsigned sc = (lane & 7) ^ (((lane >> 4) & 1) << 2), r = 16 * rp + (lane >> 3);
            c.sf.r = mk_rsrc(gw, DM * 2); c.sf.step = 64; c.sf.v0 = sc == (r & 1) ? (r & ~1u) * 2 : OOB; c.sf.v1 = c.sf.v0 + 16;
        }
        issue2<20>(c);
        { u64 pe = (u64)(ws + WS_E); asm volatile("" : "+s"(pe)); tag = *(const __attribute__((address_space(4))) u64*)pe; }
        __builtin_amdgcn_s_barrier();
        issue2<21>(c); issue2<1>(c);
        c.sq = mk_wsrc(mk_rsrc(wqkv, ff ? DM * 3 * DM * 2 : 0), 3 * DM * 2, qb * 128, 16 * rt, 64, lane);
        const unsigned r32 = lane & 31, hh = lane >> 5, q = (lane & 15) >> 2, wl = (8 * hh + q) * 128 + ((lane >> 4) & 1) * 32 + (lane & 3) * 8;
        c.xsw = hh ^ ((r32 >> 1) & 7);
#pragma unroll
        for (int u = 0; u < 3; ++u) {
            c.xf[u] = u * 65536 + (64 * rp + r32) * 128; c.xq[u] = u * 65536 + (32 * rt + r32) * 128;
            c.wf[u] = u * 65536 + half * 8192 + wl + (((pi & 1) ^ (q >> 1)) << 6); c.wq[u] = u * 65536 + wl + ((half ^ (q >> 1)) << 6);
            PIN(c.xf[u]); PIN(c.xq[u]); PIN(c.wf[u]); PIN(c.wq[u]);
        }
    }
    asm volatile("" : "+s"(tag), "+s"(out));
    tag += 0x9E3779B97F4A7C15ull; tag = (tag ^ (tag >> 30)) * 0xBF58476D1CE4E5B9ull; tag = (tag ^ (tag >> 27)) * 0x94D049BB133111EBull; tag ^= tag >> 31;
    c.tag = tag;
    f32x16 acc[4];
    const unsigned fh = cg < 14 ? WS_FH + (rb * 16 + cg) * 8 : OOB, ho = (cg < 14 ? WS_H : OOB) + ((rb * 4 + 2 * rp) * 112 + (cg * 4 + pi) * 2) * 1024 + lane * 16;
    if (wid < 4) {
        const unsigned hb = (28 * h + ((lane & 4) ? 27 : 0)) >> 6, grp = (lane >> 3) & 3;
        c.pollo = WS_FQ + ((grp == 3 ? hb : (grp == 1 ? 14 + hb : 7 + hb)) * 16 + (grp < 2 ? rt * 4 + (lane & 3) : (grp == 2 ? 0 : ab))) * 8;
        c.fqo = (tid == 0 && cg < 14) ? WS_FQ + ((cg < 7 ? 14 + cg : cg - 7) * 16 + rb) * 8 : OOB;
        phase1<0>(c, acc, cg, rb, wid);
        attn_part(ws, rws, lds, wid, lane, h, ab, tag, ho, acc, __all((c.fq[0] == (unsigned)tag) & (c.fq[1] == (unsigned)(tag >> 32))), c.pollo);
        __builtin_amdgcn_endpgm();
    }
    {
        phase1<1>(c, acc, cg, rb, wid);
        if (cg < 14) {
            u32x4 hd[4]; epi_h(acc[0], acc[1], hd); epi_h(acc[2], acc[3], hd + 2);
            PIN(hd[0]); PIN(hd[1]); PIN(hd[2]); PIN(hd[3]);
            lds_wait(lds + L_CNT + 32 + rt * 4, 1);
            store_h(hd, ho, rws);
        }
        __builtin_amdgcn_s_setprio(3);
        waitv<0>(); *(volatile LAS unsigned*)(lds + L_CNT + 64 + wid * 4) = 1u;
        if (wid == 4) {
#pragma unroll 1
            for (int spin = 0; spin < 4000000; ++spin) {
                const u32x4 v0 = *(volatile LAS u32x4*)(lds + L_CNT + 64), v1 = *(volatile LAS u32x4*)(lds + L_CNT + 80);
                if (rfl(v0[0] & v0[1] & v0[2] & v0[3] & v1[0] & v1[1] & v1[2] & v1[3])) break;
                __builtin_amdgcn_s_sleep(2);
            }
            asm volatile("" ::: "memory");
            set_flag(rws, lane == 0 ? fh : OOB, tag);
        }
        if (j < 28) out_part(oc, ws, out, lds, lane, j >> 2, tag);
        else if (b == 255 && wid == 4) {
            wait_flags<6, 20>(rws, WS_FQ + lane * 8, tag, 21 * 128);
            set_flag(rws, lane == 0 ? WS_E : OOB, tag);
        }
        __builtin_amdgcn_endpgm();
    }
}

extern "C" void kernel_launch(void* const* d_in, const int* in_sizes, int n_in, void* d_out, int out_size, void* d_ws, size_t ws_size, hipStream_t stream) {
    (void)hipFuncSetAttribute((const void*)fused, hipFuncAttributeMaxDynamicSharedMemorySize, LDS_BYTES);
    fused<<<256, 512, LDS_BYTES, stream>>>((const uint16_t*)d_in[0], (const uint16_t*)d_in[1], (const uint16_t*)d_in[2], (const uint16_t*)d_in[4], (const uint16_t*)d_in[5],
                                           (const uint16_t*)d_in[6], (const uint16_t*)d_in[7], (char*)d_ws, (uint16_t*)d_out);
}
```
